```python
import torch

def _softmax(x, axis=-1):
    return torch.softmax(x, dim=axis)
def _gelu(x):
    return torch.nn.functional.gelu(x, approximate="tanh")
def _silu(x):
    return torch.nn.functional.silu(x)
def _sigmoid(x):
    return torch.sigmoid(x)
def _layernorm(x, axis=-1, eps=1e-5):
    m = x.mean(dim=axis, keepdim=True)
    v = x.var(dim=axis, keepdim=True, unbiased=False)
    return (x - m) / torch.sqrt(v + eps)
def _scatter_add(tgt, idx, src, axis=0):
    return tgt.clone().index_add_(axis, idx, src)
def _segment_sum(src, seg, n):
    out = torch.zeros((n,) + src.shape[1:], dtype=src.dtype)
    return out.index_add_(0, seg, src)
def _pad(x, pw):
    flat = tuple(v for pair in reversed(pw) for v in pair)
    return torch.nn.functional.pad(x, flat)

kernel_name = 'gated_mix__sliding_block__moe_arch_mod__prompt'

def setup_inputs():

    _w = (lambda a: (lambda t: t.to(torch.bfloat16) if t.is_floating_point() else t)(torch.as_tensor(a)))
    def _A_setup():
        B, S, D = 1, 2048, 576
        return {
            "x": _w((lambda *s: torch.randn(*s, dtype=torch.bfloat16) * 0.05)(B, S, D)),
            "wqkv": _w((lambda *s: torch.randn(*s, dtype=torch.bfloat16) * 0.8)(D, 3 * D)),
            "wo": _w((lambda *s: torch.randn(*s, dtype=torch.bfloat16) * 0.05)(D, D)),
        }
    _A_in = _A_setup()

    def _B_setup():
        B, S, D, F, C = 1, 2048, 576, 4608, 1
        return {
            "x": _w((lambda *s: torch.randn(*s, dtype=torch.bfloat16) * 0.05)(B, S, D)),
            "w_r": _w((lambda *s: torch.randn(*s, dtype=torch.bfloat16) * 3.2)(D)),
            "w_gate": _w((lambda *s: torch.randn(*s, dtype=torch.bfloat16) * 0.2)(D, F)),
            "w_up": _w((lambda *s: torch.randn(*s, dtype=torch.bfloat16) * 0.2)(D, F)),
            "w_down": _w((lambda *s: torch.randn(*s, dtype=torch.bfloat16) * 0.2)(F, D)),
        }
    _B_in = _B_setup()

    return {
        "A_x": _A_in["x"],
        "A_wqkv": _A_in["wqkv"],
        "A_wo": _A_in["wo"],
        "B_x": _B_in["x"],
        "B_w_r": _B_in["w_r"],
        "B_w_gate": _B_in["w_gate"],
        "B_w_up": _B_in["w_up"],
        "B_w_down": _B_in["w_down"],
        "gate_w": _w((lambda *s: torch.randn(*s, dtype=torch.bfloat16) * 0.05)(576) * 16.0),
    }

def reference(A_x, A_wqkv, A_wo, B_x, B_w_r, B_w_gate, B_w_up, B_w_down, gate_w):
    def _A_ref(x, wqkv, wo):
        B, S, D = x.shape
        H = 32; Dh = D // H; W = 256
        qkv = (lambda x, *d: x.permute(*d))((x @ wqkv).reshape(B, S, 3, H, Dh), 2, 0, 3, 1, 4)
        q, k, v = qkv[0], qkv[1], qkv[2]
        scores = torch.einsum("bhqd,bhkd->bhqk", q, k) * (Dh ** -0.5)
        idx = torch.arange(S)
        mask = (idx[:, None] - idx[None, :] >= 0) & (idx[:, None] - idx[None, :] < W)
        scores = torch.where(mask[None, None], scores, -1e9)
        p = _softmax(scores, axis=-1)
        o = torch.einsum("bhqk,bhkd->bhqd", p, v)
        return (lambda x, *d: x.permute(*d))(o, 0, 2, 1, 3).reshape(B, S, D) @ wo

    def _B_ref(x, w_r, w_gate, w_up, w_down):
        B, S, D = x.shape
        C = 1
        r = x @ w_r
        idx = torch.argsort(-r, axis=-1)[:, :C]
        idx_e = (lambda x, n, axis: torch.repeat_interleave(x, n, dim=axis))(idx.reshape(B, C, 1), D, axis=2)
        x_sel = (lambda x, i, axis: torch.take_along_dim(x, i, dim=axis))(x, idx_e, axis=1)
        g = _silu(x_sel @ w_gate)
        u = x_sel @ w_up
        y = (g * u) @ w_down
        r_sel = _sigmoid((lambda x, i, axis: torch.take_along_dim(x, i, dim=axis))(r, idx, axis=1))
        y = y * r_sel.reshape(B, C, 1)
        b_off = (torch.arange(B) * S).reshape(B, 1)
        flat_idx = (idx + b_off).reshape(B * C)
        zero = (x * 0.0).reshape(B * S, D)
        upd = _scatter_add(zero, flat_idx, y.reshape(B * C, D), axis=0)
        return x + upd.reshape(B, S, D)

    x = A_x
    g = _sigmoid((x * gate_w).sum(-1, keepdims=True))
    a = _A_ref(x, A_wqkv, A_wo)
    b = _B_ref(x, B_w_r, B_w_gate, B_w_up, B_w_down)
    return g * a + (1 - g) * b
```

```cpp
#include <hip/hip_runtime.h>
#include <hip/hip_bf16.h>
typedef unsigned short u16;
__device__ __forceinline__ float b2f(u16 x){ return __uint_as_float(((unsigned)x)<<16); }
__device__ __forceinline__ u16 f2b(float f){ unsigned u=__float_as_uint(f); u += 0x7fffu + ((u>>16)&1u); return (u16)(u>>16); }
__device__ __forceinline__ float rb(float f){ return b2f(f2b(f)); }
constexpr int S=2048, D=576, F=4608, H=32, DH=18, W=256;

__global__ void k_qkv(const u16* x, const u16* w, u16* qkv){
  int n = blockIdx.x*64+threadIdx.x; int m = blockIdx.y;
  if(n>=3*D) return;
  float acc=0.f;
  for(int k=0;k<D;k++) acc += b2f(x[m*D+k])*b2f(w[k*3*D+n]);
  qkv[m*3*D+n]=f2b(acc);
}
__global__ void k_rg(const u16* x, const u16* wr, const u16* gw, float* r, float* g){
  int m = blockIdx.x; int l=threadIdx.x;
  float a=0.f,b=0.f;
  for(int k=l;k<D;k+=64){ float xv=b2f(x[m*D+k]); a+=xv*b2f(wr[k]); b+=rb(xv*b2f(gw[k])); }
  for(int o=32;o>=1;o>>=1){ a+=__shfl_xor(a,o); b+=__shfl_xor(b,o);}
  if(l==0){ r[m]=rb(a); float s=rb(b); g[m]=rb(1.f/(1.f+__expf(-s))); }
}
__global__ void k_argmax(const float* r, int* sel){
  __shared__ float sv[256]; __shared__ int si[256];
  int t=threadIdx.x; float bv=-INFINITY; int bi=0;
  for(int i=t;i<S;i+=256){ float v=r[i]; if(v>bv){bv=v;bi=i;} }
  sv[t]=bv; si[t]=bi; __syncthreads();
  if(t==0){ for(int i=1;i<256;i++){ if(sv[i]>bv || (sv[i]==bv && si[i]<bi)){bv=sv[i];bi=si[i];} } sel[0]=bi; }
}
__global__ void k_gu(const u16* x, const int* sel, const u16* wg, const u16* wu, float* h){
  int f = blockIdx.x*64+threadIdx.x; int s=sel[0];
  float a=0.f,b=0.f;
  for(int k=0;k<D;k++){ float xv=b2f(x[s*D+k]); a+=xv*b2f(wg[k*F+f]); b+=xv*b2f(wu[k*F+f]); }
  a=rb(a); b=rb(b); float sl=rb(a/(1.f+__expf(-a))); h[f]=rb(sl*b);
}
__global__ void k_down(const float* h, const u16* wd, const float* r, const int* sel, float* y){
  int n = blockIdx.x*64+threadIdx.x; float a=0.f;
  for(int f=0;f<F;f++) a+=h[f]*b2f(wd[f*D+n]);
  float rs=rb(1.f/(1.f+__expf(-r[sel[0]])));
  y[n]=rb(rb(a)*rs);
}
__global__ void k_attn(const u16* qkv, u16* o){
  int i = blockIdx.x; int h = blockIdx.y; int l=threadIdx.x;
  float q[DH]; for(int d=0;d<DH;d++) q[d]=b2f(qkv[i*3*D+h*DH+d]);
  float s[4]; float mx=-INFINITY;
  for(int t=0;t<4;t++){ int j=i-(l+64*t); float v=-INFINITY; if(j>=0){ float a=0.f; for(int d=0;d<DH;d++) a+=q[d]*b2f(qkv[j*3*D+D+h*DH+d]); v=rb(rb(a)*rb(0.23570226f)); } s[t]=v; mx=fmaxf(mx,v);}
  for(int o2=32;o2>=1;o2>>=1) mx=fmaxf(mx,__shfl_xor(mx,o2));
  float sum=0.f; for(int t=0;t<4;t++){ s[t]=__expf(s[t]-mx); sum+=s[t]; }
  for(int o2=32;o2>=1;o2>>=1) sum+=__shfl_xor(sum,o2);
  float acc[DH]; for(int d=0;d<DH;d++) acc[d]=0.f;
  for(int t=0;t<4;t++){ int j=i-(l+64*t); if(j>=0){ float p=rb(s[t]/sum); for(int d=0;d<DH;d++) acc[d]+=p*b2f(qkv[j*3*D+2*D+h*DH+d]); } }
  for(int d=0;d<DH;d++){ float a=acc[d]; for(int o2=32;o2>=1;o2>>=1) a+=__shfl_xor(a,o2); if(l==0) o[i*D+h*DH+d]=f2b(a);}
}
__global__ void k_out(const u16* o, const u16* wo, const u16* x, const float* g, const float* y, const int* sel, u16* out){
  int n = blockIdx.x*64+threadIdx.x; int m=blockIdx.y;
  float acc=0.f; for(int k=0;k<D;k++) acc+=b2f(o[m*D+k])*b2f(wo[k*D+n]);
  float a=rb(acc); float b=b2f(x[m*D+n]); if(m==sel[0]) b=rb(b+y[n]);
  float gg=g[m]; out[m*D+n]=f2b(rb(gg*a)+rb(rb(1.f-gg)*b));
}
extern "C" void kernel_launch(void* const* d_in, const int* in_sizes, int n_in, void* d_out, int out_size, void* d_ws, size_t ws_size, hipStream_t stream){
  const u16* x=(const u16*)d_in[0]; const u16* wqkv=(const u16*)d_in[1]; const u16* wo=(const u16*)d_in[2];
  const u16* wr=(const u16*)d_in[4]; const u16* wg=(const u16*)d_in[5]; const u16* wu=(const u16*)d_in[6]; const u16* wd=(const u16*)d_in[7]; const u16* gw=(const u16*)d_in[8];
  char* ws=(char*)d_ws;
  u16* qkv=(u16*)ws; u16* o=(u16*)(ws+(8<<20)); float* r=(float*)(ws+(12<<20)); float* g=r+S; float* h=g+S; float* y=h+F; int* sel=(int*)(y+D);
  k_qkv<<<dim3(27,S),64,0,stream>>>(x,wqkv,qkv);
  k_rg<<<S,64,0,stream>>>(x,wr,gw,r,g);
  k_argmax<<<1,256,0,stream>>>(r,sel);
  k_gu<<<F/64,64,0,stream>>>(x,sel,wg,wu,h);
  k_down<<<9,64,0,stream>>>(h,wd,r,sel,y);
  k_attn<<<dim3(S,H),64,0,stream>>>(qkv,o);
  k_out<<<dim3(9,S),64,0,stream>>>(o,wo,x,g,y,sel,(u16*)d_out);
}
```

```cpp
#include <hip/hip_runtime.h>
#include <hip/hip_bf16.h>

typedef unsigned long long u64;
typedef unsigned v4u __attribute__((ext_vector_type(4)));
typedef unsigned v2u __attribute__((ext_vector_type(2)));
typedef short bf16x8 __attribute__((ext_vector_type(8)));
typedef short s16x4 __attribute__((ext_vector_type(4)));
typedef float f32x16 __attribute__((ext_vector_type(16)));
typedef float f32x4 __attribute__((ext_vector_type(4)));
typedef float f32x2 __attribute__((ext_vector_type(2)));
typedef __bf16 bf2 __attribute__((ext_vector_type(2)));
#define LAS __attribute__((address_space(3)))
#define GAS __attribute__((address_space(1)))
typedef LAS char* lptr;
#define DI __device__ __forceinline__

constexpr int S = 2048, D = 576, F = 4608;
constexpr unsigned WS_EPOCH = 0;
constexpr unsigned WS_AM    = 1024;
constexpr unsigned WS_F1    = 4096;
constexpr unsigned WS_F2    = WS_F1 + 256 * 128;
constexpr unsigned WS_FH    = WS_F2 + 256 * 128;
constexpr unsigned WS_FY    = WS_FH + 256 * 128;
constexpr unsigned WS_G     = WS_FY + 32 * 128;
constexpr unsigned WS_HP    = WS_G + 2048 * 8;
constexpr unsigned WS_YP    = WS_HP + 256 * 512;
constexpr unsigned WS_QK    = 1u << 20;
constexpr unsigned WS_V     = WS_QK + 144 * 32768;
constexpr unsigned WS_OM    = WS_V + 256 * 9216;
constexpr unsigned WS_OT    = WS_OM + 64 * 32768;
constexpr unsigned WS_END   = WS_OT + 32 * 8192;
constexpr unsigned L_K   = 0;
constexpr unsigned L_V   = 30720;
constexpr unsigned L_WO  = 59392;
constexpr unsigned L_MISC = 142336;
constexpr unsigned L_GU  = L_MISC;
constexpr unsigned L_SEL = L_MISC + 2048;
constexpr unsigned L_TAG = L_MISC + 2048 + 16;
constexpr unsigned L_RED = L_MISC + 2048 + 64;
constexpr unsigned L_H   = L_MISC + 2304;
constexpr unsigned L_Y8  = L_MISC + 2816;
constexpr unsigned L_YS  = L_MISC + 4864;
constexpr unsigned LDS_BYTES = 163840;
constexpr int SPIN_CAP = 1 << 18;

#define STAMP(k) do {} while (0)

#define MFMA32(a, b, c) __builtin_amdgcn_mfma_f32_32x32x16_bf16(a, b, c, 0, 0, 0)
#define MFMA16(a, b, c) __builtin_amdgcn_mfma_f32_16x16x32_bf16(a, b, c, 0, 0, 0)
#define SBAR() __builtin_amdgcn_sched_barrier(0)
#define PIN(x) asm volatile("" : "+v"(x))
#define BAR() asm volatile("s_waitcnt lgkmcnt(0)\n\ts_barrier" ::: "memory")
#define VMW(n) asm volatile("s_waitcnt vmcnt(" #n ")" ::: "memory")
#define RFL(x) __builtin_amdgcn_readfirstlane(x)

DI unsigned cvtpk(float lo, float hi) { f32x2 v = {lo, hi}; return __builtin_bit_cast(unsigned, __builtin_convertvector(v, bf2)); }
DI float bflo(unsigned u) { return __uint_as_float(u << 16); }
DI float bfhi(unsigned u) { return __uint_as_float(u & 0xffff0000u); }
DI float rbf(float f) { return bflo(cvtpk(f, f)); }
DI float dot2(unsigned a, unsigned b, float c) { return __builtin_amdgcn_fdot2_f32_bf16(__builtin_bit_cast(bf2, a), __builtin_bit_cast(bf2, b), c, false); }
DI float sigmoidf(float z) { return __builtin_amdgcn_rcpf(1.f + __builtin_amdgcn_exp2f(-1.4426950408889634f * z)); }
DI bf16x8 asb(v4u v) { return __builtin_bit_cast(bf16x8, v); }
DI bf16x8 lds16(lptr p) { return *(LAS bf16x8*)p; }
DI s16x4 ldstr(lptr p) { return __builtin_amdgcn_ds_read_tr16_b64_v4i16((LAS s16x4*)p); }
DI bf16x8 cat(s16x4 a, s16x4 b) { return __builtin_shufflevector(a, b, 0, 1, 2, 3, 4, 5, 6, 7); }

#define DMA_ASM(FLAGS) unsigned sv; asm volatile("s_mov_b32 %0, m0\n\ts_mov_b32 m0, %3\n\ts_nop 0\n\tglobal_load_lds_dwordx4 %1, %2" FLAGS "\n\ts_mov_b32 m0, %0" : "=&s"(sv) : "v"(voff), "s"(sbase), "s"(lds) : "memory")
DI void dma(unsigned voff, const void* sbase, unsigned lds) { DMA_ASM(""); }
DI void dma_dev(unsigned voff, const void* sbase, unsigned lds) { DMA_ASM(" sc1"); }
DI void st16_dev(unsigned voff, void* sbase, v4u v) { asm volatile("global_store_dwordx4 %0, %1, %2 sc1\n\ts_nop 1" ::"v"(voff), "v"(v), "s"(sbase) : "memory"); }
DI void st8_dev(unsigned voff, void* sbase, v2u v) { asm volatile("global_store_dwordx2 %0, %1, %2 sc1" ::"v"(voff), "v"(v), "s"(sbase) : "memory"); }
DI void st4_dev(unsigned voff, void* sbase, unsigned v) { asm volatile("global_store_dword %0, %1, %2 sc1" ::"v"(voff), "v"(v), "s"(sbase) : "memory"); }
#define LD16_DEV(r, voff, sbase) asm volatile("global_load_dwordx4 %0, %1, %2 sc1" : "=v"(r) : "v"(voff), "s"(sbase) : "memory")
#define LD8_DEV(r, voff, sbase) asm volatile("global_load_dwordx2 %0, %1, %2 sc1" : "=v"(r) : "v"(voff), "s"(sbase) : "memory")
#define LD4_DEV(r, voff, sbase) asm volatile("global_load_dword %0, %1, %2 sc1" : "=v"(r) : "v"(voff), "s"(sbase) : "memory")
#define LD16_NT(r, voff, sbase) asm volatile("global_load_dwordx4 %0, %1, %2 nt" : "=v"(r) : "v"(voff), "s"(sbase) : "memory")
#define LD16(r, voff, sbase) asm volatile("global_load_dwordx4 %0, %1, %2" : "=v"(r) : "v"(voff), "s"(sbase) : "memory")
#define LD8(r, voff, sbase) asm volatile("global_load_dwordx2 %0, %1, %2" : "=v"(r) : "v"(voff), "s"(sbase) : "memory")
DI unsigned ld4_dev_wait(unsigned voff, const void* sbase) { unsigned r; asm volatile("global_load_dword %0, %1, %2 sc1\n\ts_waitcnt vmcnt(0)" : "=&v"(r) : "v"(voff), "s"(sbase) : "memory"); return r; }
DI v2u ld8_dev_wait(unsigned voff, const void* sbase) { v2u r; asm volatile("global_load_dwordx2 %0, %1, %2 sc1\n\ts_waitcnt vmcnt(0)" : "=&v"(r) : "v"(voff), "s"(sbase) : "memory"); return r; }

DI void wait_flags(unsigned voff, const void* ws, unsigned tag) {
#pragma unroll 1
    for (int spin = 0; spin < SPIN_CAP; ++spin) {
        const unsigned v = ld4_dev_wait(voff, ws);
        if (__all(v == tag)) break;
        __builtin_amdgcn_s_sleep(1);
    }
}
DI unsigned wait_argmax(v2u g, int lane, const void* ws, unsigned tag) {
#pragma unroll 1
    for (int spin = 0; spin < SPIN_CAP; ++spin) {
        if (__all(g.y == tag)) break;
        __builtin_amdgcn_s_sleep(1);
        g = ld8_dev_wait(WS_AM + (lane & 31) * 8, ws);
    }
    unsigned k = g.x;
#pragma unroll
    for (int o = 16; o >= 1; o >>= 1) { const unsigned t = (unsigned)__shfl_xor((int)k, o); k = k > t ? k : t; }
    return RFL(k);
}

template <bool TFORM>
DI void gemm_loop(lptr lds, int lane, int wm, int wn, f32x16 (&acc)[2][2]) {
    const int r = lane & 31, h = lane >> 5, z = (r >> 1) & 7;
    const int blk = (lane >> 4) & 1, q = (lane & 15) >> 2, p = lane & 3;
    unsigned xo[4], wo_[2];
#pragma unroll
    for (int s = 0; s < 4; ++s) xo[s] = (64 * wm + r) * 128 + 16 * ((2 * s + h) ^ z);
#pragma unroll
    for (int t = 0; t < 2; ++t) wo_[t] = 16384 + 2048 * h + 1024 * wn + 64 * (4 * t + q) + 16 * ((2 * blk + (p >> 1)) ^ (2 * h + t)) + 8 * (p & 1);
#pragma unroll 1
    for (int s = 0; s < 9; ++s) {
        BAR();
        lptr b = lds + (s & 3) * 32768;
#pragma unroll
        for (int k = 0; k < 4; ++k) {
            const bf16x8 x0 = lds16(b + xo[k]), x1 = lds16(b + xo[k] + 4096);
            const bf16x8 w0 = cat(ldstr(b + wo_[0] + 4096 * k), ldstr(b + wo_[1] + 4096 * k));
            const bf16x8 w1 = cat(ldstr(b + wo_[0] + 4096 * k + 512), ldstr(b + wo_[1] + 4096 * k + 512));
            if (TFORM) {
                acc[0][0] = MFMA32(w0, x0, acc[0][0]); acc[1][0] = MFMA32(w0, x1, acc[1][0]);
                acc[0][1] = MFMA32(w1, x0, acc[0][1]); acc[1][1] = MFMA32(w1, x1, acc[1][1]);
            } else {
                acc[0][0] = MFMA32(x0, w0, acc[0][0]); acc[1][0] = MFMA32(x1, w0, acc[1][0]);
                acc[0][1] = MFMA32(x0, w1, acc[0][1]); acc[1][1] = MFMA32(x1, w1, acc[1][1]);
            }
        }
    }
}

template <bool HALF>
DI void load_stage(const char* xb, const char* wb, int ks, unsigned slot, const unsigned (&xv)[4], const unsigned (&wv)[4], int lw) {
    const char* xs = xb + ks * 128;
    const char* wsb = wb + ks * (64 * 3456);
#pragma unroll
    for (int i = 0; i < 4; ++i) dma(xv[i], xs, slot + (lw * 4 + i) * 1024);
#pragma unroll
    for (int i = 0; i < 4; ++i) if (!HALF || !(i & 1)) dma(wv[i], wsb, slot + 16384 + (lw * 4 + i) * 1024);
}
template <bool HALF>
DI v2u loader_loop(const char* xb, const char* wb, int r0, unsigned lds0, int lane, int lw, const char* ws) {
    unsigned xv[4], wv[4];
#pragma unroll
    for (int i = 0; i < 4; ++i) {
        const int row = 8 * (4 * lw + i) + (lane >> 3);
        xv[i] = row * 1152 + 16 * ((lane & 7) ^ ((row >> 1) & 7));
        const int wr = 8 * (2 * lw + (i >> 1)) + ((lane >> 2) & 7);
        wv[i] = wr * 3456 + 16 * (4 * (2 * (i & 1) + (lane >> 5)) + ((lane & 3) ^ ((wr >> 2) & 3)));
    }
    int ks = r0;
#define NEXT_STAGE(sl) do { load_stage<HALF>(xb, wb, ks, lds0 + (sl) * 32768, xv, wv, lw); ks = (ks == 8) ? 0 : ks + 1; } while (0)
#define LW(nf, nh) do { if (HALF) VMW(nh); else VMW(nf); asm volatile("s_barrier" ::: "memory"); } while (0)
    NEXT_STAGE(0); NEXT_STAGE(1); NEXT_STAGE(2); NEXT_STAGE(3);
    LW(24, 18);
#pragma unroll 1
    for (int s = 1; s < 6; ++s) { LW(16, 12); NEXT_STAGE((s - 1) & 3); }
    v2u am; LD8_DEV(am, WS_AM + (lane & 31) * 8, ws);
    LW(17, 13); LW(9, 7); LW(1, 1);
    asm volatile("s_waitcnt vmcnt(0)" : "+v"(am)::"memory");
    return am;
}

__global__ void __launch_bounds__(512, 2)
fused_kernel(const char* __restrict__ x, const char* __restrict__ wqkv, const char* __restrict__ wo, const char* __restrict__ wr,
             const char* __restrict__ wg, const char* __restrict__ wu, const char* __restrict__ wd, const char* __restrict__ gw,
             char* __restrict__ out, char* __restrict__ ws) {
    __shared__ __attribute__((aligned(16))) char smem[LDS_BYTES];
    const lptr lds = (lptr)smem;
    const unsigned lds0 = (unsigned)(size_t)smem;
    const int lane = threadIdx.x & 63, wid = RFL(threadIdx.x >> 6);
    const int b = blockIdx.x, xcd = b & 7, j = b >> 3;
    STAMP(0);
    const bool isfree = j >= 28;
    const int fi = xcd * 4 + (j - 28);
    const int gi = xcd * 28 + j;
    const int arb = isfree ? (fi >> 4) : 2 + gi / 16, hp = isfree ? (fi & 15) : (gi & 15);
    const int r32 = lane & 31, hh = lane >> 5;
    unsigned tag, selkey;
    v4u dw[21];

    if (!isfree) {
        const int rl = j / 7, cl = j - rl * 7;
        const int rb = (xcd >> 1) * 4 + rl, ct = (xcd & 1) * 7 + cl;
        if (wid < 4) {
            const int wm = wid & 1, wn = wid >> 1;
            tag = RFL(ld4_dev_wait(WS_EPOCH, ws)) + 1;
            if (wid == 0 && lane == 0) *(LAS unsigned*)(lds + L_TAG) = tag;
            f32x16 acc[2][2] = {};
            const bool idle = (ct == 13 && wn == 1);
            if (idle) {
#pragma unroll 1
                for (int s = 0; s < 9; ++s) BAR();
            }
            else if (ct < 9) gemm_loop<true>(lds, lane, wm, wn, acc);
            else gemm_loop<false>(lds, lane, wm, wn, acc);
            STAMP(1);
            if (!idle) {
                if (ct < 9) {
                    const int col0 = ct * 128 + wn * 64;
                    const float sc = col0 < 576 ? 0.23570226039551584f * 1.4426950408889634f : 1.f;
#pragma unroll
                    for (int tm = 0; tm < 2; ++tm)
#pragma unroll
                        for (int tn = 0; tn < 2; ++tn) {
                            unsigned d[8];
#pragma unroll
                            for (int i = 0; i < 8; ++i) d[i] = cvtpk(acc[tm][tn][2 * i] * sc, acc[tm][tn][2 * i + 1] * sc);
#pragma unroll
                            for (int g = 0; g < 4; g += 2) {
                                auto s0 = __builtin_amdgcn_permlane32_swap(d[2 * g], d[2 * g + 2], false, false);
                                auto s1 = __builtin_amdgcn_permlane32_swap(d[2 * g + 1], d[2 * g + 3], false, false);
                                const unsigned off = WS_QK + (unsigned)((col0 >> 3) + tn * 4 + g + hh) * 32768u + (rb * 128 + wm * 64 + tm * 32 + r32) * 16;
                                st16_dev(off, ws, (v4u){s0[0], s1[0], s0[1], s1[1]});
                            }
                        }
                } else {
                    const int col0 = (ct - 9) * 128 + wn * 64;
#pragma unroll
                    for (int tm = 0; tm < 2; ++tm)
#pragma unroll
                        for (int tn = 0; tn < 2; ++tn)
#pragma unroll
                            for (int s = 0; s < 2; ++s) {
                                v4u d;
#pragma unroll
                                for (int i = 0; i < 4; ++i) d[i] = cvtpk(acc[tm][tn][8 * s + 2 * i], acc[tm][tn][8 * s + 2 * i + 1]);
                                const unsigned off = WS_V + (unsigned)((rb * 8 + wm * 4 + tm * 2 + s) * 2 + hh) * 9216u + (col0 + tn * 32 + r32) * 16;
                                st16_dev(off, ws, d);
                            }
                }
            }
            BAR();
            STAMP(2);
            VMW(0);
            BAR();
            STAMP(3);
            if (wid == 0 && lane == 0) st4_dev(WS_F1 + (rb * 16 + ct) * 128, ws, tag);
        } else {
            const int lw = wid - 4;
            const char* xb = x + (size_t)rb * (128 * 1152);
            const char* wb = wqkv + ct * 256;
            const int r0 = (cl + 2 * rl) % 9;
            v2u am;
            if (ct == 13) am = loader_loop<true>(xb, wb, r0, lds0, lane, lw, ws);
            else am = loader_loop<false>(xb, wb, r0, lds0, lane, lw, ws);
            tag = RFL(*(LAS unsigned*)(lds + L_TAG));
            STAMP(1);
            selkey = wait_argmax(am, lane, ws, tag);
            if (lw == 0 && lane == 0) *(LAS unsigned*)(lds + L_SEL) = selkey;
            if (gi >= 216) { BAR(); BAR(); }
            else {
                const int tc = gi / 3, tt = gi - tc * 3;
                const int g4 = lane >> 4, k0 = 192 * tt + 48 * lw + 12 * g4;
                const int sel = 0xffff - (selkey & 0xffff);
                v4u xa; v2u xb2; v4u w[12];
                LD16(xa, sel * 1152 + k0 * 2, x); LD8(xb2, sel * 1152 + k0 * 2 + 16, x);
                const char* src = (((lane >> 3) & 1) ? wu : wg) + (size_t)k0 * 9216 + tc * 128 + (lane & 7) * 16;
                BAR();
#pragma unroll
                for (int i = 0; i < 12; ++i) { const char* pp = src + i * 9216; asm volatile("global_load_dwordx4 %0, %1, off nt" : "=v"(w[i]) : "v"(pp) : "memory"); }
                BAR();
                STAMP(3);
                asm volatile("s_waitcnt vmcnt(0)" : "+v"(xa), "+v"(xb2), "+v"(w[0]), "+v"(w[1]), "+v"(w[2]), "+v"(w[3]), "+v"(w[4]), "+v"(w[5]),
                             "+v"(w[6]), "+v"(w[7]), "+v"(w[8]), "+v"(w[9]), "+v"(w[10]), "+v"(w[11])::"memory");
                STAMP(4);
                const unsigned xs[6] = {xa.x, xa.y, xa.z, xa.w, xb2.x, xb2.y};
                const int i16 = lane & 15;
                f32x4 a = {};
#pragma unroll
                for (int i = 0; i < 12; ++i) {
                    const unsigned v16 = (i & 1) ? (xs[i >> 1] >> 16) : (xs[i >> 1] & 0xffffu);
                    const unsigned val = (i16 & 1) ? (v16 << 16) : v16;
                    v4u f;
#pragma unroll
                    for (int rr = 0; rr < 4; ++rr) f[rr] = (i16 >> 1) == rr ? val : 0u;
                    a = MFMA16(asb(f), asb(w[i]), a);
                }
                if (lane < 32) *(LAS f32x4*)(lds + L_GU + lw * 512 + ((i16 >> 3) * 64 + 8 * (i16 & 7) + 4 * g4) * 4) = a;
            }
        }
    } else {
        const int row0 = ((xcd >> 1) * 8 + (xcd & 1) * 4 + (j - 28)) * 64;
        const int row = row0 + wid * 8 + (lane >> 3);
        v4u xr[9], w1[9], w2[9];
        unsigned tagv; LD4_DEV(tagv, WS_EPOCH, ws);
#pragma unroll
        for (int i = 0; i < 9; ++i) {
            LD16(xr[i], row * 1152 + i * 128 + (lane & 7) * 16, x);
            LD16(w1[i], i * 128 + (lane & 7) * 16, wr);
            LD16(w2[i], i * 128 + (lane & 7) * 16, gw);
        }
        asm volatile("s_waitcnt vmcnt(0)" : "+v"(tagv), "+v"(xr[0]), "+v"(xr[1]), "+v"(xr[2]), "+v"(xr[3]), "+v"(xr[4]), "+v"(xr[5]), "+v"(xr[6]), "+v"(xr[7]), "+v"(xr[8])::"memory");
        asm volatile("" : "+v"(w1[0]), "+v"(w1[1]), "+v"(w1[2]), "+v"(w1[3]), "+v"(w1[4]), "+v"(w1[5]), "+v"(w1[6]), "+v"(w1[7]), "+v"(w1[8]));
        asm volatile("" : "+v"(w2[0]), "+v"(w2[1]), "+v"(w2[2]), "+v"(w2[3]), "+v"(w2[4]), "+v"(w2[5]), "+v"(w2[6]), "+v"(w2[7]), "+v"(w2[8]));
        tag = RFL(tagv) + 1;
        float ra = 0.f, ga = 0.f;
#pragma unroll
        for (int i = 0; i < 9; ++i)
#pragma unroll
            for (int e = 0; e < 4; ++e) { ra = dot2(xr[i][e], w1[i][e], ra); ga = dot2(xr[i][e], w2[i][e], ga); }
#pragma unroll
        for (int o = 1; o < 8; o <<= 1) { ra += __shfl_xor(ra, o); ga += __shfl_xor(ga, o); }
        const unsigned rb16 = cvtpk(ra, ra) & 0xffffu;
        unsigned key = ((rb16 & 0x8000u ? (~rb16 & 0xffffu) : (rb16 | 0x8000u)) << 16) | (0xffffu - row);
#pragma unroll
        for (int o = 8; o < 64; o <<= 1) { const unsigned t = (unsigned)__shfl_xor((int)key, o); key = key > t ? key : t; }
        if (lane == 0) *(LAS unsigned*)(lds + L_RED + wid * 4) = key;
        if ((lane & 7) == 0) st8_dev(WS_G + row * 8, ws, (v2u){__float_as_uint(sigmoidf(rbf(ga))), tag});
        BAR();
        if (wid == 0) {
            unsigned k2 = *(LAS unsigned*)(lds + L_RED + (lane & 7) * 4);
#pragma unroll
            for (int o = 1; o < 8; o <<= 1) { const unsigned t = (unsigned)__shfl_xor((int)k2, o); k2 = k2 > t ? k2 : t; }
            if (lane == 0) st8_dev(WS_AM + fi * 8, ws, (v2u){k2, tag});
        }
        BAR();
        STAMP(1);
        const unsigned dbase = (unsigned)(144 * fi + (lane >> 3)) * 1152u + (lane & 7) * 16;
#pragma unroll
        for (int i = 0; i < 18; ++i) LD16_NT(dw[i], dbase + i * 9216 + wid * 128, wd);
#pragma unroll
        for (int i = 0; i < 3; ++i) { const int rg = (wid + 8 * i < 18) ? wid + 8 * i : wid; LD16_NT(dw[18 + i], dbase + rg * 9216 + 1024, wd); }
        asm volatile("s_waitcnt vmcnt(0)" : "+v"(dw[0]), "+v"(dw[1]), "+v"(dw[2]), "+v"(dw[3]), "+v"(dw[4]), "+v"(dw[5]), "+v"(dw[6]), "+v"(dw[7]), "+v"(dw[8]), "+v"(dw[9]), "+v"(dw[10])::"memory");
        asm volatile("" : "+v"(dw[11]), "+v"(dw[12]), "+v"(dw[13]), "+v"(dw[14]), "+v"(dw[15]), "+v"(dw[16]), "+v"(dw[17]), "+v"(dw[18]), "+v"(dw[19]), "+v"(dw[20]));
        if (wid == 0) {
            v2u am = ld8_dev_wait(WS_AM + (lane & 31) * 8, ws);
            selkey = wait_argmax(am, lane, ws, tag);
            if (lane == 0) *(LAS unsigned*)(lds + L_SEL) = selkey;
        }
        STAMP(3);
    }

    const int g0 = (36 * hp) >> 3;
    const int qt = wid & 3, hd = wid >> 2;
    if (wid == 7 && lane < 48) *(LAS v4u*)(lds + L_V + (lane * 37 + 36) * 16) = (v4u){0x3f803f80u, 0x3f803f80u, 0x3f803f80u, 0x3f803f80u};
    if (wid == 0) {
        const int kind = lane < 2 ? 0 : (lane < 8 ? 1 : 2), idx = lane < 2 ? lane : (lane < 8 ? lane - 2 : lane - 8);
        const int drb = kind == 0 ? 0 : (idx >> 1), side = idx & 1;
        int col = kind < 2 ? kind * 576 + (g0 + 4 * side) * 8 : 1152 + 36 * hp + 35 * side;
        int prb = arb - drb;
        if (lane >= 14 || prb < 0) { prb = arb; col = g0 * 8; }
        wait_flags(WS_F1 + (prb * 16 + (col >> 7)) * 128, ws, tag);
    }
    BAR();
    STAMP(5);
    if (!isfree && wid == 4 && gi < 216) {
        f32x4 a = *(LAS f32x4*)(lds + L_GU + r32 * 16);
#pragma unroll
        for (int i = 1; i < 4; ++i) a += *(LAS f32x4*)(lds + L_GU + i * 512 + r32 * 16);
        if (lane < 32) st16_dev(WS_HP + gi * 512 + lane * 16, ws, __builtin_bit_cast(v4u, a));
    }
    v4u q0, q1;
    {
#pragma unroll
        for (int i = 0; i < 4; ++i) {
            const int n = wid + 8 * i;
            if (n < 30) {
                const int g = n / 6, part = n - g * 6;
                if (arb * 2 - 4 + part >= 0) dma_dev(WS_QK + (unsigned)(72 + g0 + g) * 32768u + ((arb * 2 - 4 + part) * 64 + lane) * 16, ws, RFL(lds0 + L_K + n * 1024));
            }
        }
#pragma unroll
        for (int i = 0; i < 4; ++i) {
            const int n = wid + 8 * i;
            if (n < 28) {
                const int c = n * 64 + lane, hb = (c * 1772) >> 16, cc = c - hb * 37, tgh = arb * 16 - 32 + hb;
                if (cc < 36 && hb < 48 && tgh >= 0) dma_dev(WS_V + (unsigned)tgh * 9216u + (36 * hp + cc) * 16, ws, RFL(lds0 + L_V + n * 1024));
            }
        }
    }
    {
        const unsigned qo = WS_QK + (unsigned)(g0 + 2 * hd) * 32768u + (arb * 128 + qt * 32 + r32) * 16;
        LD16_DEV(q0, qo + hh * 32768u, ws); LD16_DEV(q1, qo + 2 * 32768u, ws);
    }
    const int rt = xcd * 4 + (j >> 3), cq = j & 7;
#pragma unroll
    for (int i = 0; i < 11; ++i) {
        int n = wid + 8 * i; if (i == 10 && n > 80) n = 80;
        const int c = n * 64 + lane, row = (c * 7282) >> 16, cc = c - row * 9;
        dma(row * 1152 + cq * 144 + cc * 16, wo, RFL(lds0 + L_WO + n * 1024));
    }
    f32x16 mA, mB;
#pragma unroll
    for (int r = 0; r < 16; ++r) { const int key = (r & 3) + 8 * (r >> 2) + 4 * hh; mA[r] = key <= r32 ? 0.f : -INFINITY; mB[r] = key > r32 ? 0.f : -INFINITY; }
    asm volatile("s_waitcnt vmcnt(11)" : "+v"(q0), "+v"(q1)::"memory");
    if (!isfree && wid == 4 && gi < 216 && lane == 0) st4_dev(WS_FH + gi * 128, ws, tag);
    {
        const int a0 = (36 * hp + 18 * hd) & 7;
#pragma unroll
        for (int e = 0; e < 4; ++e) {
            if (hh == 0 && 2 * e < a0) q0[e] = 0u;
            if (hh == 1 || 2 * e >= a0 + 2) q1[e] = 0u;
        }
    }
    BAR();
    STAMP(6);
    f32x16 o;
    {
        const lptr kp0 = lds + L_K + (2 * hd + hh) * 6144 + (qt * 32 + r32) * 16;
        const lptr kp1 = lds + L_K + (2 * hd + 2) * 6144 + (qt * 32 + r32) * 16;
        const int vdim = r32 < 16 ? ((r32 & 3) + ((r32 >> 3) << 2) + ((r32 & 4) << 1)) : r32;
        const int vd = (r32 == 18 || r32 == 22) ? 36 : 18 * hd + vdim;
        const lptr vp = lds + L_V + (qt * 4 + hh) * 592 + vd * 16;
        const int imin = (8 - arb * 4 - qt) > 0 ? (8 - arb * 4 - qt) : 0;
        const bf16x8 bq0 = asb(q0), bq1 = asb(q1);
        const f32x16 z = {};
        o = z;
        f32x16 sa, sb;
        bf16x8 k0 = lds16(kp0 + 8 * 512), k1 = lds16(kp1 + 8 * 512);
        sa = MFMA32(k0, bq0, mA); sa = MFMA32(k1, bq1, sa);
        k0 = lds16(kp0 + 7 * 512); k1 = lds16(kp1 + 7 * 512);
#define EX2(s, r) cvtpk(__builtin_amdgcn_exp2f(s[r]), __builtin_amdgcn_exp2f(s[r + 1]))
#define STEP(i, SC, SN, CN) { \
        const bf16x8 v0 = lds16(vp + (i) * 2368), v1 = lds16(vp + (i) * 2368 + 1184); v4u p0, p1; SBAR(); \
        SN = MFMA32(k0, bq0, CN); p0[0] = EX2(SC, 0); p0[1] = EX2(SC, 2); PIN(p0); SBAR(); \
        SN = MFMA32(k1, bq1, SN); p0[2] = EX2(SC, 4); p0[3] = EX2(SC, 6); PIN(p0); SBAR(); \
        if ((i) >= 2) { k0 = lds16(kp0 + ((i) - 2) * 512); k1 = lds16(kp1 + ((i) - 2) * 512); } \
        p1[0] = EX2(SC, 8); p1[1] = EX2(SC, 10); PIN(p1); SBAR(); \
        o = MFMA32(v0, asb(p0), o); p1[2] = EX2(SC, 12); p1[3] = EX2(SC, 14); PIN(p1); SBAR(); \
        o = MFMA32(v1, asb(p1), o); }
        do {
            if (imin == 8) break;
            STEP(8, sa, sb, z); if (imin == 7) { sa = sb; break; }
            STEP(7, sb, sa, z); if (imin == 6) break;
            STEP(6, sa, sb, z); if (imin == 5) { sa = sb; break; }
            STEP(5, sb, sa, z); if (imin == 4) break;
            STEP(4, sa, sb, z); if (imin == 3) { sa = sb; break; }
            STEP(3, sb, sa, z); if (imin == 2) break;
            STEP(2, sa, sb, z); if (imin == 1) { sa = sb; break; }
            STEP(1, sb, sa, mB);
        } while (0);
        {
            const bf16x8 v0 = lds16(vp + imin * 2368), v1 = lds16(vp + imin * 2368 + 1184);
            const v4u p0 = {EX2(sa, 0), EX2(sa, 2), EX2(sa, 4), EX2(sa, 6)}, p1 = {EX2(sa, 8), EX2(sa, 10), EX2(sa, 12), EX2(sa, 14)};
            o = MFMA32(v0, asb(p0), o); o = MFMA32(v1, asb(p1), o);
        }
        if (__builtin_expect(__any(!(o[10] >= 0x1p-64f && o[10] <= 0x1p64f)), 0)) {
            float m = -INFINITY;
            o = z;
#pragma unroll 1
            for (int it = imin - 9; it <= 8; ++it) {
                if (it < imin && it >= 0) it = imin;
                const int i = it < 0 ? it + 9 : it;
                if (it == imin) m = fmaxf(m, __shfl_xor(m, 32));
                f32x16 s = MFMA32(lds16(kp0 + i * 512), bq0, z); s = MFMA32(lds16(kp1 + i * 512), bq1, s);
#pragma unroll
                for (int r = 0; r < 16; ++r) {
                    const int key = (r & 3) + 8 * (r >> 2) + 4 * hh;
                    if ((i == 8 && key > r32) || (i == 0 && key <= r32)) s[r] = -INFINITY;
                }
                if (it < 0) {
#pragma unroll
                    for (int r = 0; r < 16; ++r) m = fmaxf(m, s[r]);
                } else {
#pragma unroll
                    for (int r = 0; r < 16; ++r) s[r] -= m;
                    const v4u p0 = {EX2(s, 0), EX2(s, 2), EX2(s, 4), EX2(s, 6)}, p1 = {EX2(s, 8), EX2(s, 10), EX2(s, 12), EX2(s, 14)};
                    o = MFMA32(lds16(vp + i * 2368), asb(p0), o); o = MFMA32(lds16(vp + i * 2368 + 1184), asb(p1), o);
                }
            }
        }
    }
    STAMP(7);
    {
        const float inv = 1.f / o[10];
        v4u d;
#pragma unroll
        for (int i = 0; i < 4; ++i) d[i] = cvtpk(o[2 * i] * inv, o[2 * i + 1] * inv);
        const int tok = arb * 128 + qt * 32 + r32, head = 2 * hp + hd;
        st16_dev(WS_OM + (unsigned)(2 * head + hh) * 32768u + tok * 16, ws, d);
        if (hh == 0) st4_dev(WS_OT + head * 8192 + tok * 4, ws, cvtpk(o[8] * inv, o[9] * inv));
    }
    VMW(0);
    BAR();
    STAMP(8);
    if (wid == 0 && lane == 0) st4_dev(WS_F2 + (arb * 16 + hp) * 128, ws, tag);
    selkey = RFL(*(LAS unsigned*)(lds + L_SEL));
    const int sel = 0xffff - (selkey & 0xffff);

    if (isfree) {
        const int f0 = 144 * fi, c0 = f0 >> 6;
        if (wid < 3) {
            int c = c0 + lane / 3; if (lane >= 12 || c > ((f0 + 143) >> 6)) c = c0;
            wait_flags(WS_FH + (c * 3 + lane % 3) * 128, ws, tag);
            const int fl = wid * 64 + lane, f = f0 + (fl < 144 ? fl : 0);
            unsigned pv[6];
#pragma unroll
            for (int i = 0; i < 6; ++i) LD4_DEV(pv[i], WS_HP + ((f >> 6) * 3 + (i >> 1)) * 512 + ((i & 1) * 64 + (f & 63)) * 4, ws);
            asm volatile("s_waitcnt vmcnt(0)" : "+v"(pv[0]), "+v"(pv[1]), "+v"(pv[2]), "+v"(pv[3]), "+v"(pv[4]), "+v"(pv[5])::"memory");
            const float g = rbf(__uint_as_float(pv[0]) + __uint_as_float(pv[2]) + __uint_as_float(pv[4]));
            const float u = rbf(__uint_as_float(pv[1]) + __uint_as_float(pv[3]) + __uint_as_float(pv[5]));
            const float hv = rbf(g * sigmoidf(g)) * u;
            if (fl < 144) *(LAS unsigned short*)(lds + L_H + fl * 2) = (unsigned short)cvtpk(hv, hv);
        }
        BAR();
        const int i16 = lane & 15;
        f32x4 a = {}, a8 = {};
#pragma unroll
        for (int i = 0; i < 21; ++i) {
            const int rg = i < 18 ? i : wid + 8 * (i - 18);
            unsigned v16 = *(LAS unsigned short*)(lds + L_H + ((rg < 18 ? rg : 0) * 8 + (lane >> 3)) * 2);
            if (rg >= 18) v16 = 0;
            const unsigned val = (i16 & 1) ? (v16 << 16) : v16;
            v4u f;
#pragma unroll
            for (int rr = 0; rr < 4; ++rr) f[rr] = ((i16 & 7) >> 1) == rr ? val : 0u;
            if (i < 18) a = MFMA16(asb(f), asb(dw[i]), a); else a8 = MFMA16(asb(f), asb(dw[i]), a8);
        }
#pragma unroll
        for (int e = 0; e < 4; ++e) { a[e] += __shfl(a[e], (lane + 40) & 63); a8[e] += __shfl(a8[e], (lane + 40) & 63); }
        const bool own = lane < 32 && i16 < 8;
        const int yc = 8 * i16 + 4 * (lane >> 4);
        if (own) { st16_dev(WS_YP + fi * 2304 + (wid * 64 + yc) * 4, ws, __builtin_bit_cast(v4u, a)); *(LAS f32x4*)(lds + L_Y8 + wid * 256 + yc * 4) = a8; }
        BAR();
        if (wid == 0 && lane < 16) {
            f32x4 s = *(LAS f32x4*)(lds + L_Y8 + lane * 16);
#pragma unroll
            for (int i = 1; i < 8; ++i) s += *(LAS f32x4*)(lds + L_Y8 + i * 256 + lane * 16);
            st16_dev(WS_YP + fi * 2304 + 2048 + lane * 16, ws, __builtin_bit_cast(v4u, s));
        }
        VMW(0);
        BAR();
        if (wid == 0 && lane == 0) st4_dev(WS_FY + fi * 128, ws, tag);
        STAMP(9);
    }

    const int t16 = lane & 15, g4 = lane >> 4, kh = wid >> 2;
    const int tok = rt * 64 + (wid & 3) * 16 + t16;
    v2u xv[3], gg;
#pragma unroll
    for (int i = 0; i < 3; ++i) { int c = 16 * (3 * kh + i) + 4 * g4; if (c > 68) c = 68; LD8(xv[i], tok * 1152 + (cq * 72 + c) * 2, x); }
    LD8_DEV(gg, WS_G + tok * 8, ws);
    bf16x8 wf[9][5];
    const lptr wp = lds + L_WO + (18 * (16 * kh + (g4 >> 1)) + 8 * (g4 & 1) + (t16 >> 2)) * 144 + (lane & 3) * 8;
    const lptr wt = lds + L_WO + (18 * (16 * kh + 4 * g4 + (t16 >> 3)) + 16 + ((t16 >> 2) & 1)) * 144 + (lane & 3) * 8;
#define WFRAG(s) _Pragma("unroll") for (int c = 0; c < 5; ++c) wf[s][c] = cat(ldstr(wp + (s) * (36 * 144) + c * 32), ldstr(wp + (s) * (36 * 144) + c * 32 + 4 * 144));
    WFRAG(0) WFRAG(1) WFRAG(2) WFRAG(3) WFRAG(4) WFRAG(5)
    asm volatile("s_waitcnt vmcnt(0)" : "+v"(xv[0]), "+v"(xv[1]), "+v"(xv[2]), "+v"(gg)::"memory");
    if (wid == 0) wait_flags(WS_F2 + ((rt >> 1) * 16 + (lane & 15)) * 128, ws, tag);
    BAR();
    STAMP(10);
    f32x4 acc[5] = {};
    {
        v4u of[9];
#pragma unroll
        for (int s = 0; s < 8; ++s) LD16_DEV(of[s], WS_OM + (unsigned)(32 * kh + 4 * s + g4) * 32768u + tok * 16, ws);
        unsigned ot[4];
#pragma unroll
        for (int e = 0; e < 4; ++e) LD4_DEV(ot[e], WS_OT + (16 * kh + 4 * g4 + e) * 8192 + tok * 4, ws);
#define OSTEP(s, n) asm volatile("s_waitcnt vmcnt(" #n ")" : "+v"(of[s])::"memory"); \
        _Pragma("unroll") for (int c = 0; c < 5; ++c) acc[c] = MFMA16(wf[s][c], asb(of[s]), acc[c]);
        OSTEP(0, 11) SBAR(); WFRAG(6) OSTEP(1, 10) SBAR(); WFRAG(7) OSTEP(2, 9) SBAR();
#pragma unroll
        for (int c = 0; c < 5; ++c) wf[8][c] = cat(ldstr(wt + c * 32), ldstr(wt + c * 32 + 36 * 144));
        OSTEP(3, 8) OSTEP(4, 7) OSTEP(5, 6) OSTEP(6, 5) OSTEP(7, 4)
        asm volatile("s_waitcnt vmcnt(0)" : "+v"(ot[0]), "+v"(ot[1]), "+v"(ot[2]), "+v"(ot[3])::"memory");
        of[8] = (v4u){ot[0], ot[1], ot[2], ot[3]};
#pragma unroll
        for (int c = 0; c < 5; ++c) acc[c] = MFMA16(wf[8][c], asb(of[8]), acc[c]);
    }
    STAMP(11);
    if (kh == 0) {
#pragma unroll
        for (int c = 3; c < 5; ++c) *(LAS f32x4*)(lds + ((wid & 3) * 5 + c) * 1024 + lane * 16) = acc[c];
    } else {
#pragma unroll
        for (int c = 0; c < 3; ++c) *(LAS f32x4*)(lds + ((wid & 3) * 5 + c) * 1024 + lane * 16) = acc[c];
    }
    const bool selwg = (sel >> 6) == rt;
    if (selwg) {
        wait_flags(WS_FY + (wid * 4 + (lane & 3)) * 128, ws, tag);
        v2u y[4];
        const int yl = lane < 36 ? lane : 0;
#pragma unroll
        for (int i = 0; i < 4; ++i) LD8_DEV(y[i], WS_YP + (wid * 4 + i) * 2304 + cq * 288 + yl * 8, ws);
        asm volatile("s_waitcnt vmcnt(0)" : "+v"(y[0]), "+v"(y[1]), "+v"(y[2]), "+v"(y[3])::"memory");
        f32x2 s = __builtin_bit_cast(f32x2, y[0]) + __builtin_bit_cast(f32x2, y[1]) + __builtin_bit_cast(f32x2, y[2]) + __builtin_bit_cast(f32x2, y[3]);
        if (lane < 36) *(LAS f32x2*)(lds + L_YS + wid * 288 + lane * 8) = s;
    }
#pragma unroll 1
    for (int spin = 0; spin < SPIN_CAP && !__all(gg.y == tag); ++spin) gg = ld8_dev_wait(WS_G + tok * 8, ws);
    BAR();
    STAMP(12);
    {
        const float g = __uint_as_float(gg.x), g1 = 1.f - g;
        const unsigned rk = selkey >> 16, rb16 = (rk & 0x8000u) ? (rk & 0x7fffu) : (~rk & 0xffffu);
        const float rs = rbf(sigmoidf(bflo(rb16)));
#pragma unroll
        for (int i = 0; i < 3; ++i) {
            if (i == 2 && kh == 1) break;
            const int ctile = 3 * kh + i, c = 16 * ctile + 4 * g4;
            const f32x4 other = *(LAS f32x4*)(lds + ((wid & 3) * 5 + ctile) * 1024 + lane * 16);
            const f32x4 a = (kh == 0 ? acc[i] : acc[i < 2 ? 3 + i : 4]) + other;
            f32x4 bb = {bflo(xv[i].x), bfhi(xv[i].x), bflo(xv[i].y), bfhi(xv[i].y)};
            if (selwg && tok == sel && c < 72) {
                f32x4 y = {};
#pragma unroll
                for (int w = 0; w < 8; ++w) y += *(LAS f32x4*)(lds + L_YS + w * 288 + c * 4);
#pragma unroll
                for (int e = 0; e < 4; ++e) bb[e] += rbf(y[e]) * rs;
            }
            const v2u r = {cvtpk(g * a[0] + g1 * bb[0], g * a[1] + g1 * bb[1]), cvtpk(g * a[2] + g1 * bb[2], g * a[3] + g1 * bb[3])};
            if (c < 72) *(GAS v2u*)(out + tok * 1152 + (cq * 72 + c) * 2) = r;
        }
    }
    STAMP(13);
    if (b == 0 && wid == 7) {
#pragma unroll 1
        for (int i = 0; i < 4; ++i) wait_flags(WS_F2 + (i * 64 + lane) * 128, ws, tag);
        if (lane == 0) st4_dev(WS_EPOCH, ws, tag);
    }
}

extern "C" void kernel_launch(void* const* d_in, const int* in_sizes, int n_in, void* d_out, int out_size, void* d_ws, size_t ws_size,
                              hipStream_t stream) {
    fused_kernel<<<256, 512, 0, stream>>>((const char*)d_in[0], (const char*)d_in[1], (const char*)d_in[2], (const char*)d_in[4],
                                          (const char*)d_in[5], (const char*)d_in[6], (const char*)d_in[7], (const char*)d_in[8],
                                          (char*)d_out, (char*)d_ws);
}
```

```cpp
#include <hip/hip_runtime.h>
#include <hip/hip_bf16.h>

typedef unsigned long long u64;
typedef unsigned v4u __attribute__((ext_vector_type(4)));
typedef unsigned v2u __attribute__((ext_vector_type(2)));
typedef short bf16x8 __attribute__((ext_vector_type(8)));
typedef short s16x4 __attribute__((ext_vector_type(4)));
typedef float f32x16 __attribute__((ext_vector_type(16)));
typedef float f32x4 __attribute__((ext_vector_type(4)));
typedef float f32x2 __attribute__((ext_vector_type(2)));
typedef __bf16 bf2 __attribute__((ext_vector_type(2)));
#define LAS __attribute__((address_space(3)))
#define CAS __attribute__((address_space(4)))
typedef LAS char* lptr;
#define DI __device__ __forceinline__

constexpr unsigned WS_EPOCH = 0;
constexpr unsigned WS_AM    = 4096;
constexpr unsigned WS_F1    = 8192;
constexpr unsigned WS_F2    = WS_F1 + 256 * 128;
constexpr unsigned WS_FC    = WS_F2 + 256 * 128;
constexpr unsigned WS_G     = WS_FC + 256 * 128;
constexpr unsigned WS_HP    = WS_G + 2048 * 8;
constexpr unsigned WS_YP    = WS_HP + 18 * 4 * 4096;
constexpr unsigned WS_QK    = 1u << 20;
constexpr unsigned WS_V     = WS_QK + 144 * 32768;
constexpr unsigned WS_OM    = WS_V + 256 * 9216;
constexpr unsigned WS_OT    = WS_OM + 64 * 32768;
constexpr unsigned L_K    = 0;
constexpr unsigned L_V    = 30720;
constexpr unsigned L_WO   = 61440;
constexpr unsigned L_GU   = L_WO + 40960;
constexpr unsigned L_MISC = L_WO + 92160;
constexpr unsigned L_RV   = L_MISC;
constexpr unsigned L_X    = L_MISC;
constexpr unsigned L_G    = L_X + 9216;
constexpr unsigned L_Y    = L_V + 592;
constexpr unsigned L_TAG  = L_G + 512;
constexpr unsigned C_KEY = L_TAG + 4, C_K4 = L_TAG + 16, C_TILE = L_TAG + 32, C_ATT = L_TAG + 36, C_KV = L_TAG + 40, C_GU = L_TAG + 44, C_Y = L_TAG + 48, C_RT = L_TAG + 52,
                   C_LATE = L_TAG + 56, C_GO = L_TAG + 60, C_YG = L_TAG + 64, C_WO = L_TAG + 68, C_ALL = L_TAG + 72, C_EX = L_TAG + 96;
constexpr unsigned LDS_BYTES = 163840;
constexpr int SPIN_CAP = 1 << 18;
constexpr int CODE_STEP = 120;
constexpr int RF = 216;
constexpr int RH = 648 - 2 * RF;
static_assert(RF % 24 == 0 && RH % 24 == 0 && RF <= 312 && RH <= 312 && RH * 16 <= 3 * 26 * 64, "gate/up split");
constexpr unsigned EMPTY = 0xffffffffu;

#define MFMA32(a, b, c) __builtin_amdgcn_mfma_f32_32x32x16_bf16(a, b, c, 0, 0, 0)
#define MFMA16(a, b, c) __builtin_amdgcn_mfma_f32_16x16x32_bf16(a, b, c, 0, 0, 0)
#define SBAR() __builtin_amdgcn_sched_barrier(0)
#define PIN(x) asm volatile("" : "+v"(x))
#define BAR() asm volatile("s_waitcnt lgkmcnt(0)\n\ts_barrier" ::: "memory")
#define VMW_(n) asm volatile("s_waitcnt vmcnt(" #n ")" ::: "memory")
#define VMW(n) VMW_(n)
#define RFL(x) __builtin_amdgcn_readfirstlane(x)
#define DPP(x, c) ((unsigned)__builtin_amdgcn_mov_dpp((int)(x), c, 0xf, 0xf, true))

DI unsigned cvtpk(float lo, float hi) { f32x2 v = {lo, hi}; return __builtin_bit_cast(unsigned, __builtin_convertvector(v, bf2)); }
DI float bflo(unsigned u) { return __uint_as_float(u << 16); }
DI float bfhi(unsigned u) { return __uint_as_float(u & 0xffff0000u); }
DI float rbf(float f) { return bflo(cvtpk(f, f)); }
DI float sigmoidf(float z) { return __builtin_amdgcn_rcpf(1.f + __builtin_amdgcn_exp2f(-1.4426950408889634f * z)); }
DI bf16x8 asb(v4u v) { return __builtin_bit_cast(bf16x8, v); }
DI bf16x8 lds16(lptr p) { return *(LAS bf16x8*)p; }
DI s16x4 ldstr(lptr p) { return __builtin_amdgcn_ds_read_tr16_b64_v4i16((LAS s16x4*)p); }
DI bf16x8 cat(s16x4 a, s16x4 b) { return __builtin_shufflevector(a, b, 0, 1, 2, 3, 4, 5, 6, 7); }
DI unsigned umax(unsigned a, unsigned b) { return a > b ? a : b; }
DI unsigned umin(unsigned a, unsigned b) { return a < b ? a : b; }
DI unsigned wave_umax(unsigned k) {
    k = umax(k, DPP(k, 0x128)); k = umax(k, DPP(k, 0x124)); k = umax(k, DPP(k, 0x4E)); k = umax(k, DPP(k, 0xB1));
    return umax(umax(__builtin_amdgcn_readlane(k, 0), __builtin_amdgcn_readlane(k, 16)), umax(__builtin_amdgcn_readlane(k, 32), __builtin_amdgcn_readlane(k, 48)));
}

DI void dma(unsigned voff, const void* sbase, unsigned lds) {
    unsigned sv;
    asm volatile("s_mov_b32 %0, m0\n\ts_mov_b32 m0, %3\n\ts_nop 0\n\tglobal_load_lds_dwordx4 %1, %2\n\ts_mov_b32 m0, %0"
                 : "=&s"(sv) : "v"(voff), "s"(sbase), "s"(lds) : "memory");
}
DI void st16_dev(unsigned voff, void* sbase, v4u v) { asm volatile("global_store_dwordx4 %0, %1, %2 sc1\n\ts_nop 1" ::"v"(voff), "v"(v), "s"(sbase) : "memory"); }
DI void st8_dev(unsigned voff, void* sbase, v2u v) { asm volatile("global_store_dwordx2 %0, %1, %2 sc1" ::"v"(voff), "v"(v), "s"(sbase) : "memory"); }
DI void st4_dev(unsigned voff, void* sbase, unsigned v) { asm volatile("global_store_dword %0, %1, %2 sc1" ::"v"(voff), "v"(v), "s"(sbase) : "memory"); }
#define LD16_DEV(r, voff, sbase) asm volatile("global_load_dwordx4 %0, %1, %2 sc1" : "=v"(r) : "v"(voff), "s"(sbase) : "memory")
#define LD4_DEV(r, voff, sbase) asm volatile("global_load_dword %0, %1, %2 sc1" : "=v"(r) : "v"(voff), "s"(sbase) : "memory")
#define LD16(r, voff, sbase) asm volatile("global_load_dwordx4 %0, %1, %2" : "=v"(r) : "v"(voff), "s"(sbase) : "memory")
DI unsigned ld4_dev_wait(unsigned voff, const void* sbase) { unsigned r; asm volatile("global_load_dword %0, %1, %2 sc1\n\ts_waitcnt vmcnt(0)" : "=&v"(r) : "v"(voff), "s"(sbase) : "memory"); return r; }
DI v2u ld8_dev_wait(unsigned voff, const void* sbase) { v2u r; asm volatile("global_load_dwordx2 %0, %1, %2 sc1\n\ts_waitcnt vmcnt(0)" : "=&v"(r) : "v"(voff), "s"(sbase) : "memory"); return r; }
DI v4u ld16_dev_wait(unsigned voff, const void* sbase) { v4u r; asm volatile("global_load_dwordx4 %0, %1, %2 sc1\n\ts_waitcnt vmcnt(0)" : "=&v"(r) : "v"(voff), "s"(sbase) : "memory"); return r; }

DI unsigned read_tag(const void* ws) { unsigned r; asm volatile("s_load_dword %0, %1, 0x0\n\ts_waitcnt lgkmcnt(0)" : "=&s"(r) : "s"(ws) : "memory"); return r + 1; }

DI void wait_flags(unsigned v, unsigned voff, const void* ws, unsigned tag) {
    if (__builtin_expect(!__all(v == tag), 0)) {
        int spin = 0;
        _Pragma("unroll 1") do { __builtin_amdgcn_s_sleep(3); if (v != tag) v = ld4_dev_wait(voff, ws); } while (!__all(v == tag) && ++spin < SPIN_CAP);
    }
}
DI v2u wait8(v2u g, unsigned voff, const void* ws, unsigned tag) {
    if (__builtin_expect(!__all(g.y == tag), 0)) {
        int spin = 0;
        _Pragma("unroll 1") do { __builtin_amdgcn_s_sleep(3); if (g.y != tag) g = ld8_dev_wait(voff, ws); } while (!__all(g.y == tag) && ++spin < SPIN_CAP);
    }
    return g;
}
DI v4u wait16(v4u g, unsigned voff, const void* ws, unsigned tag) {
    if (__builtin_expect(!__all(g.y == tag && g.w == tag), 0)) {
        int spin = 0;
        _Pragma("unroll 1") do { __builtin_amdgcn_s_sleep(3); if (g.y != tag || g.w != tag) g = ld16_dev_wait(voff, ws); } while (!__all(g.y == tag && g.w == tag) && ++spin < SPIN_CAP);
    }
    return g;
}
DI void poll_flags(unsigned voff, const void* ws, unsigned tag) {
    wait_flags(ld4_dev_wait(voff, ws), voff, ws, tag);
}

#define LANE0(op) "s_mov_b64 %[sv], exec\n\ts_mov_b64 exec, 1\n\t" op "\n\ts_mov_b64 exec, %[sv]"
DI void arrive(lptr cnt) { u64 sv; asm volatile(LANE0("ds_add_u32 %1, %2") : [sv] "=&s"(sv) : "v"(cnt), "v"(1u) : "memory"); }
DI bool last_of(lptr cnt, unsigned n) {
    unsigned old; u64 sv;
    asm volatile(LANE0("ds_add_rtn_u32 %0, %2, %3") "\n\ts_waitcnt lgkmcnt(0)" : "=&v"(old), [sv] "=&s"(sv) : "v"(cnt), "v"(1u) : "memory");
    return RFL(old) == n - 1;
}
DI void flag_dev(unsigned voff, void* sbase, unsigned v) { u64 sv; asm volatile(LANE0("global_store_dword %1, %2, %3 sc1") : [sv] "=&s"(sv) : "v"(voff), "v"(v), "s"(sbase) : "memory"); }
DI unsigned yslot(unsigned i) { const unsigned r = (i * 86) >> 8; return L_Y + r * 640 + (i - 3 * r) * 16; }
DI unsigned lds_word(lptr p) { return RFL(*(volatile LAS unsigned*)p); }
template <unsigned n> DI void wait_count(lptr cnt) {
    unsigned v, c, spin = -SPIN_CAP;
    asm volatile("1:\n\tds_read_b32 %0, %3\n\ts_waitcnt lgkmcnt(0)\n\tv_readfirstlane_b32 %1, %0\n\ts_cmp_eq_u32 %1, %4\n\ts_cbranch_scc1 2f\n\t"
                 "s_add_u32 %2, %2, 1\n\ts_sleep 1\n\ts_cbranch_scc0 1b\n2:" : "=&v"(v), "=&s"(c), "+s"(spin) : "v"(cnt), "n"(n) : "memory", "scc");
}

struct Fr { bf16x8 x[4], w[4]; };
template <bool TFORM>
DI void put8(const f32x4& a, const f32x4& b, float sc, unsigned off, char* ws) {
    if (TFORM) {
        auto s0 = __builtin_amdgcn_permlane16_swap(cvtpk(a[0] * sc, a[1] * sc), cvtpk(b[0] * sc, b[1] * sc), false, false);
        auto s1 = __builtin_amdgcn_permlane16_swap(cvtpk(a[2] * sc, a[3] * sc), cvtpk(b[2] * sc, b[3] * sc), false, false);
        st16_dev(off, ws, (v4u){s0[0], s1[0], s0[1], s1[1]});
    } else {
        auto s0 = __builtin_amdgcn_permlane32_swap(cvtpk(a[0], a[1]), cvtpk(b[0], b[1]), false, false);
        auto s1 = __builtin_amdgcn_permlane32_swap(cvtpk(a[2], a[3]), cvtpk(b[2], b[3]), false, false);
        st16_dev(off, ws, (v4u){s0[0], s1[0], s0[1], s1[1]});
    }
}
template <int MODE>
DI void gemm_tile(lptr lds, int lane, int wm, int wn, int rb, int ct, u64 kord, char* ws) {
    const int t16 = lane & 15, g = lane >> 4, q = t16 >> 2, p = lane & 3;
    const int rot = MODE == 2 ? 2 * wn : 0;
    lptr xo[2][2][2], wo_[2][2][2];
#pragma unroll
    for (int k = 0; k < 2; ++k)
#pragma unroll
        for (int u = 0; u < 2; ++u) {
            xo[0][k][u] = lds + (64 * wm + 16 * ((2 * u) ^ rot) + t16) * 128 + 16 * ((4 * k + g) ^ (t16 >> 1));
            wo_[0][k][u] = lds + 16384 + 2048 * g + (MODE == 2 ? 512 : 1024) * wn + 64 * (4 * u + q) + 16 * (2 * (k ^ (g & 1)) + ((p >> 1) ^ u)) + 8 * (p & 1);
            xo[1][k][u] = xo[0][k][u] + 65536; wo_[1][k][u] = wo_[0][k][u] + 65536;
            if (MODE == 2 || u == 0) { PIN(xo[0][k][u]); PIN(xo[1][k][u]); }
            PIN(wo_[0][k][u]); PIN(wo_[1][k][u]);
        }
    const unsigned vb = L_RV + (lane & 1) * 1152 + g * 16;
    unsigned vo;
#define RX(f, i, b, k) f.x[i] = lds16((MODE == 2 ? xo[(b) >> 1][k][(i) >> 1] + ((i) & 1) * 2048 : xo[(b) >> 1][k][0] + (i) * 2048) + ((b) & 1) * 32768)
#define WA(i, b, k, t) (wo_[(b) >> 1][(i) & 1][t] + ((b) & 1) * 32768 + 8192 * (k) + 512 * ((i) >> 1))
#define RW(f, i, b, k) do { \
        if (MODE != 2 || (i) < 2) f.w[i] = cat(ldstr(WA(i, b, k, 0)), ldstr(WA(i, b, k, 1))); \
        else if ((i) == 2) f.w[2] = lds16(lds + vo + 64 * (k)); } while (0)
#define MX(tm, tn, X, W) do { \
        if (MODE == 0) acc[tm][tn] = MFMA16(W, X, acc[tm][tn]); \
        else if (MODE == 1 || (tn) < 2) acc[tm][tn] = MFMA16(X, W, acc[tm][tn]); \
        else if ((tn) == 2 && (tm) < 2) acc[tm][2] = MFMA16(W, X, acc[tm][2]); \
        SBAR(); } while (0)
#define MM(tm, tn, f) MX(tm, tn, f.x[tm], f.w[tn])
#define SUB(ON, f, n, b, k) do { \
        if (ON) MM(0, 0, f); RX(n, 0, b, k); SBAR(); if (ON) MM(1, 0, f); RX(n, 1, b, k); SBAR(); \
        if (ON) MM(2, 0, f); RX(n, 2, b, k); SBAR(); if (ON) MM(3, 0, f); RX(n, 3, b, k); SBAR(); \
        if (ON) MM(0, 1, f); RW(n, 0, b, k); SBAR(); if (ON) { MM(1, 1, f); MM(2, 1, f); } RW(n, 1, b, k); SBAR(); \
        if (ON) { MM(3, 1, f); MM(0, 2, f); } RW(n, 2, b, k); SBAR(); if (ON) { MM(1, 2, f); MM(2, 2, f); } RW(n, 3, b, k); SBAR(); \
        if (ON) { MM(3, 2, f); MM(0, 3, f); MM(1, 3, f); MM(2, 3, f); MM(3, 3, f); } } while (0)
    f32x4 acc[4][4] = {};
    Fr c, f;
#pragma unroll
    for (int s = 0; s < 8; ++s) {
        BAR();
        if (s >= 1) asm volatile("s_nop 15\n\ts_nop 15");
        if (s >= 1 && s <= 5) asm volatile("s_nop 15");
        if (s >= 2 && s <= 5) asm volatile("s_nop 15");
        vo = vb + ((unsigned)(kord >> (4 * s)) & 15) * 128;
        SUB(s, c, f, s & 3, 0);
        SUB(true, f, c, s & 3, 1);
    }
    unsigned off;
    float sc = 1.f;
    if (MODE == 0) {
        const int col0 = ct * 128 + wn * 64;
        if (col0 < 576) sc = 0.23570226039551584f * 1.4426950408889634f;
        off = WS_QK + (unsigned)((col0 >> 3) + (g >> 1)) * 32768u + (rb * 128 + wm * 64 + (lane & 31)) * 16;
    } else off = WS_V + (unsigned)((rb * 8 + wm * 4) * 2 + (g & 1)) * 9216u + ((ct - 9) * 128 + wn * (MODE == 2 ? 32 : 64) + 16 * (g >> 1) + t16) * 16;
#define PUT(hf, i) do { \
        if (MODE == 0) put8<true>(acc[2 * ((i) & 1)][2 * (hf) + ((i) >> 1)], acc[2 * ((i) & 1) + 1][2 * (hf) + ((i) >> 1)], sc, \
                                  off + (4 * (hf) + 2 * ((i) >> 1)) * 32768u + ((i) & 1) * 512, ws); \
        else put8<false>(acc[i][2 * (hf)], acc[i][2 * (hf) + 1], sc, off + ((i) ^ rot) * 18432u + (hf) * 512, ws); \
        SBAR(); } while (0)
    Fr e;
    vo = vb + ((unsigned)(kord >> 32) & 15) * 128;
    BAR();
    MM(0, 0, c); RX(f, 0, 0, 0); SBAR(); MM(1, 0, c); RX(f, 1, 0, 0); SBAR(); MM(2, 0, c); RX(f, 2, 0, 0); SBAR(); MM(3, 0, c); RX(f, 3, 0, 0); SBAR();
    MM(0, 1, c); RW(f, 0, 0, 0); SBAR(); MM(1, 1, c); MM(2, 1, c); RW(f, 1, 0, 0); SBAR(); MM(3, 1, c);
    MM(0, 2, c); RX(e, 0, 0, 1); SBAR(); MM(1, 2, c); MM(2, 2, c); RX(e, 1, 0, 1); SBAR(); MM(3, 2, c);
    MM(0, 3, c); RX(e, 2, 0, 1); SBAR(); MM(1, 3, c); MM(2, 3, c); RX(e, 3, 0, 1); SBAR(); MM(3, 3, c);
    if (MODE == 2) {
        RW(f, 2, 0, 0); RW(e, 2, 0, 1); RW(e, 0, 0, 1); RW(e, 1, 0, 1); SBAR();
        MM(0, 0, f); MM(1, 0, f); MM(2, 0, f); MM(3, 0, f);
        MM(0, 2, f); MM(1, 2, f); MM(0, 2, e); MM(1, 2, e);
        MM(0, 1, f); MM(1, 1, f); MM(2, 1, f); MM(3, 1, f);
        MM(0, 0, e); MM(1, 0, e); MM(2, 0, e); MM(3, 0, e); MM(0, 1, e); MM(1, 1, e); MM(2, 1, e); MM(3, 1, e);
        const unsigned tag = read_tag(ws);
        const int row = rb * 128 + wm * 64 + wn * 32 + (lane & 31);
        const float rs = g & 1 ? acc[1][2][0] : acc[0][2][0], gs = g & 1 ? acc[1][2][1] : acc[0][2][1];
        const unsigned rb16 = cvtpk(rs, rs) & 0xffffu;
        const unsigned key = wave_umax(((rb16 & 0x8000u ? (~rb16 & 0xffffu) : (rb16 | 0x8000u)) << 16) | (0xffffu - row));
        if (lane == 0) *(LAS unsigned*)(lds + C_K4 + (wm * 2 + wn) * 4) = key;
        if (last_of(lds + C_RT, 4)) {
            const v4u k4 = *(LAS v4u*)(lds + C_K4);
            if (lane == 0) st8_dev(WS_AM + rb * 128, ws, (v2u){umax(umax(k4.x, k4.y), umax(k4.z, k4.w)), tag});
        }
        PUT(0, 0); PUT(0, 1); PUT(0, 2); PUT(0, 3);
        const unsigned gt = __float_as_uint(sigmoidf(rbf(gs)));
        const unsigned gn = DPP(gt, 0xB1);
        if (lane < 32 && !(lane & 1)) st16_dev(WS_G + row * 8, ws, (v4u){gt, tag, gn, tag});
    } else {
        MM(0, 0, f); RW(e, 0, 0, 1); SBAR(); MM(1, 0, f); MM(2, 0, f); RW(e, 1, 0, 1); SBAR(); MM(3, 0, f);
        MM(0, 1, f); RW(f, 2, 0, 0); SBAR(); MM(1, 1, f); MM(2, 1, f); RW(f, 3, 0, 0); SBAR(); MM(3, 1, f);
        MM(0, 0, e); RW(e, 2, 0, 1); SBAR(); MM(1, 0, e); MM(0, 1, e); RW(e, 3, 0, 1); SBAR(); MM(1, 1, e); MM(2, 0, e); MM(3, 0, e); MM(2, 1, e); MM(3, 1, e);
        MM(0, 2, f); MM(1, 2, f); MM(2, 2, f); MM(3, 2, f);
        PUT(0, 0); MM(0, 3, f); MM(1, 3, f); MM(2, 3, f); MM(3, 3, f);
        PUT(0, 1); MM(0, 2, e); MM(1, 2, e); MM(0, 3, e); MM(1, 3, e);
        PUT(0, 2); MM(2, 2, e); MM(3, 2, e); MM(2, 3, e); MM(3, 3, e);
        PUT(0, 3); PUT(1, 0); PUT(1, 1); PUT(1, 2); PUT(1, 3);
    }
}

#define LDX(i, o) "global_load_lds_dwordx4 %[x" #i "], s[84:85] offset:" #o "\n\t"
#define LDW(i, o) "global_load_lds_dwordx4 %[w" #i "], s[86:87] offset:" #o "\n\t"
#define LOADER(WLOADS, N2, N1) asm volatile( \
        "s_mov_b32 s91, m0\n\ts_mov_b64 s[92:93], %[ko]\n\ts_mov_b32 s89, 0\n\ts_mov_b32 s94, 0\n" \
        "1:\n\ts_and_b32 s88, s92, 15\n\ts_lshr_b64 s[92:93], s[92:93], 4\n\ts_lshl_b32 s90, s88, 7\n\t" \
        "s_add_u32 m0, s89, %[sl]\n\ts_mul_i32 s95, s88, 0x36000\n\ts_waitcnt lgkmcnt(0)\n\ts_add_u32 s84, s80, s90\n\ts_addc_u32 s85, s81, 0\n\t" \
        LDX(0, 0) LDX(1, 1024) LDX(2, 2048) LDX(3, 3072) \
        "s_add_u32 m0, m0, 0x4000\n\ts_add_u32 s86, s82, s95\n\ts_addc_u32 s87, s83, 0\n\ts_sub_u32 s86, s86, 3072\n\ts_subb_u32 s87, s87, 0\n\t" \
        WLOADS \
        "s_add_u32 s89, s89, 0x8000\n\ts_and_b32 s89, s89, 0x18000\n\ts_add_u32 s94, s94, 1\n\ts_cmp_lt_u32 s94, 3\n\ts_cbranch_scc1 1b\n\t" \
        "s_waitcnt vmcnt(" #N2 ")\n\ts_barrier\n\ts_cmp_lt_u32 s94, 9\n\ts_cbranch_scc1 1b\n\t" \
        "s_waitcnt vmcnt(" #N1 ")\n\ts_barrier\n\ts_waitcnt vmcnt(0)\n\ts_barrier\n\ts_mov_b32 m0, s91" \
        :: "{s[80:81]}"(x_), "{s[82:83]}"(wqkv_), [ko] "s"(kord), [sl] "s"(slot), [x0] "v"(xv[0]), [x1] "v"(xv[1]), [x2] "v"(xv[2]), [x3] "v"(xv[3]), \
           [w0] "v"(wq[0]), [w1] "v"(wq[1]), [w2] "v"(wq[2]), [w3] "v"(wq[3]) \
        : "memory", "scc", "s84", "s85", "s86", "s87", "s88", "s89", "s90", "s91", "s92", "s93", "s94", "s95")

template <int FIRST, int COUNT, int M0 = 0, int M1 = (COUNT + 3) / 4>
DI void wo_blocks(int w, int rot, const unsigned (&o)[5], const char* wo, unsigned lds0) {
#pragma unroll
    for (int m = M0; m < M1; ++m) {
        int blk = w + 4 * m;
        if (4 * m + 4 <= COUNT || blk < COUNT) {
            blk += rot; if (blk >= COUNT) blk -= COUNT;
            blk += FIRST;
#pragma unroll
            for (int i = 0; i < 5; ++i) dma(o[i], wo + blk * (32 * 1152), RFL(lds0 + L_WO + (blk * 5 + i) * 1024));
        }
    }
}

DI int first_task(int row) { const int t = row / 648, q = row - t * 648; return 3 * t + (q >= 2 * RF ? 2 : (q >= RF ? 1 : 0)); }

__global__ void __launch_bounds__(768, 3)
fused_kernel(const char* __restrict__ x, const char* __restrict__ wqkv, char* __restrict__ ws, const char* __restrict__ wg,
             const char* __restrict__ wu, const char* __restrict__ wr, const char* __restrict__ gw, const char* __restrict__ wo,
             const char* __restrict__ wd, char* __restrict__ out) {
    u64 x_, wqkv_; unsigned k2;
    asm volatile("s_load_dwordx2 %0, %3, 0x0\n\ts_load_dwordx2 %1, %3, 0x8\n\ts_load_dword %2, %3, 0x40"
                 : "=&{s[80:81]}"(x_), "=&{s[82:83]}"(wqkv_), "=&s"(k2) : "s"(__builtin_amdgcn_kernarg_segment_ptr()));
    __shared__ __attribute__((aligned(16))) char smem[LDS_BYTES];
    const lptr lds = (lptr)smem;
    const unsigned lds0 = (unsigned)(size_t)smem;
    int lane = threadIdx.x & 63;
    int hw = RFL(threadIdx.x >> 6);
    int b = blockIdx.x, xcd = b & 7, j = b >> 3;
    unsigned pc_ = 0;
    if (hw * 32 + j < 28) {
        u64 pc; asm volatile("s_getpc_b64 %0" : "=s"(pc));
        asm volatile("global_load_dword %0, %1, %2" : "=v"(pc_) : "v"(((j & 7) * 64 + lane) * CODE_STEP), "s"(pc) : "memory");
    }
    int j6 = (j * 43) >> 8, c6 = j - j6 * 6, r1 = 2 * (xcd & 1) + (j & 1), s1 = (j >> 1) & 1, s0 = xcd & 1;
    asm volatile("" : "+s"(j6), "+s"(c6), "+s"(r1), "+s"(s1), "+s"(s0));
    const int rl = j < 24 ? j6 : r1, cl = j < 24 ? c6 : 6;
    const int rb = (xcd >> 1) * 4 + rl, ct = cl + 7 * (j < 24 ? s0 : s1);
    const unsigned k3 = (0x210210u >> (4 * ((0x24924924u >> (2 * (cl + 2 * rl))) & 3))) & 0xfff;
    const unsigned kp = (unsigned)(0x864612189864ull >> (6 * xcd)) & 63, kb2 = kp >> 4;
    const u64 kord = (u64)((kp * 0x100401u) & 0x3003003u) * 0x333u + (u64)k3 * 0x1001001u;
    unsigned tag;
    if (hw < 4 && j < 28) {
        unsigned xv[4], wq[4];
        xv[0] = (__umul24(lane >> 3, 1152) + (rb * 128 + hw * 32) * 1152) | (((lane ^ (lane >> 4)) & 7) << 4);
        wq[0] = (__umul24((lane >> 2) & 7, 3456) + (hw * 16 * 3456 + ct * 256 + 3072)) | ((lane & 32) << 1) | (((lane ^ ((lane >> 4) & 1)) & 3) << 4);
        PIN(xv[0]); PIN(wq[0]);
        xv[1] = (xv[0] ^ 64) + 8192; xv[2] = xv[0] + 16384; xv[3] = (xv[0] ^ 64) + 24576;
        wq[1] = wq[0] - 896; wq[2] = (wq[0] ^ 32) + 25600; wq[3] = (wq[0] ^ 32) + 24704;
        PIN(xv[1]); PIN(xv[2]); PIN(xv[3]); PIN(wq[1]); PIN(wq[2]); PIN(wq[3]);
        const unsigned slot = lds0 + hw * 4096;
        if (__builtin_expect(j < 26, 1)) {
            LOADER(LDW(0, 0) LDW(1, 1024) LDW(2, 2048) LDW(3, 3072), 16, 8);
        } else {
            if (!(hw & 1) || lane < 8) dma((hw & 1) * 1024 + lane * 16, hw & 2 ? gw : wr, RFL(lds0 + L_RV + (hw >> 1) * 1152 + (hw & 1) * 1024));
            LOADER(LDW(0, 0) LDW(2, 2048), 12, 6);
        }
    }
    asm volatile("s_waitcnt lgkmcnt(0)" : "+s"(b), "+s"(hw), "+v"(lane) : "s"(x_), "s"(wqkv_), "s"(k2), "v"(pc_));
    xcd = b & 7; j = b >> 3;
    const int wid = hw < 4 ? hw + 8 : hw - 4;
    const bool full = j < 26, half = (j >> 1) == 13;
    int r32 = lane & 31, hh = lane >> 5;
    const int rt = xcd * 4 + (j >> 3), cq = j & 7;
    const int hp = 2 * xcd + (j & 1), arb = full ? 15 - (j >> 1) : (half ? 0 : 16 - (j >> 1)), g0 = (36 * hp) >> 3;

    const int gp = xcd * 2 + (half ? j & 1 : (j >> 1) & 1), gs = half ? 2 : j & 1, gi = 3 * gp + gs;
    const int grow = gp * 648 + gs * RF, gg0 = (grow * 58255) >> 25, gk0 = grow - gg0 * 576;
#define GU_ADDR(w) unsigned go = (unsigned)(gk0 + 2 * (w) + hh) * 9216u + gg0 * 512 + (lane & 31) * 16
#define GU_LOAD(NP) do { \
        _Pragma("unroll") for (int i = 0; i < (NP); ++i) { \
            if (i && i * 24 == 576 - gk0) go += 512 - 576 * 9216; \
            if (!half && i) __builtin_amdgcn_s_sleep(12); \
            LD16(wv[2 * i], go, wg); LD16(wv[2 * i + 1], go, wu); go += 24 * 9216; } } while (0)
#define GET_KEY() do { \
        const unsigned amo = WS_AM + (lane & 15) * 128; \
        const v2u am_ = wait8(ld8_dev_wait(amo, ws), amo, ws, tag); \
        const unsigned key_ = wave_umax(am_.x); \
        if (lane == 0) *(LAS unsigned*)(lds + C_KEY) = key_; } while (0)
#define GU_MUL(NP, w, MID) do { \
        const char* xr_ = x + (0xffff - (lds_word(lds + C_KEY) & 0xffff)) * 1152; \
        unsigned xs[NP]; \
        _Pragma("unroll") for (int i = 0; i < (NP); ++i) { \
            int k_ = gk0 + 2 * (w) + 24 * i; if (k_ >= 576) k_ -= 576; \
            xs[i] = *(const CAS unsigned*)(xr_ + 2 * k_); } \
        v4u m; \
        _Pragma("unroll") for (int rr = 0; rr < 4; ++rr) { \
            const unsigned h_ = 0x0100 + hh * 0x0202; \
            m[rr] = ((lane & 15) >> 1) == 4 * ((lane >> 4) & 1) + rr ? (lane & 1 ? h_ << 16 | 0x0c0c : h_ | 0x0c0c0000) : 0x0c0c0c0cu; } \
        PIN(m); \
        VMW(0); SBAR(); \
        _Pragma("unroll") for (int i = 0; i < 2 * (NP); ++i) PIN(wv[i]); \
        MID; \
        f32x4 ag = {}, au_ = {}; \
        lptr p = lds + L_GU + (w) * 2048 + hh * 1024 + (lane & 15) * 64 + ((lane >> 4) & 1) * 32; \
        _Pragma("unroll") for (int i = 0; i <= (NP); ++i) { \
            if (i == (NP) || (i && i * 24 == 576 - gk0)) { \
                *(LAS f32x4*)p = (f32x4){ag[0], au_[0], ag[1], au_[1]}; *(LAS f32x4*)(p + 16) = (f32x4){ag[2], au_[2], ag[3], au_[3]}; \
                p += 24576; ag = (f32x4){}; au_ = (f32x4){}; \
            } \
            if (i == (NP)) break; \
            const bf16x8 a_ = asb((v4u){PERM(xs[i], m[0]), PERM(xs[i], m[1]), PERM(xs[i], m[2]), PERM(xs[i], m[3])}); \
            ag = MFMA16(a_, asb(wv[2 * i]), ag); au_ = MFMA16(a_, asb(wv[2 * i + 1]), au_); \
        } } while (0)
#define PERM(v, sel) __builtin_amdgcn_perm(v, v, sel)
#define GU_PUBLISH(seg, w) do { \
        { \
            f32x2 s = {}; \
            _Pragma("unroll") for (int v = 0; v < 12; ++v) s += *(LAS f32x2*)(lds + L_GU + ((seg) * 12 + v) * 2048 + ((w) * 64 + lane) * 8); \
            const int g = gg0 + (seg); \
            st16_dev(WS_HP + ((g * 4 + gi - first_task(g * 576)) * 256 + (w) * 64 + lane) * 16, ws, (v4u){__float_as_uint(s[0]), tag, __float_as_uint(s[1]), tag}); \
        } } while (0)
#define POLL_ADDR(fo) do { \
        const int kind = lane < 4 ? 0 : (lane < 10 ? 1 : 2), idx = lane < 4 ? lane : (lane < 10 ? lane - 4 : lane - 10); \
        const int side = idx & 1; \
        int col = kind < 2 ? kind * 576 + (g0 + 4 * side) * 8 : 1152 + 36 * hp + 35 * side; \
        int prb = arb - (kind == 0 ? 0 : (idx >> 1)); \
        if (lane >= 16 || prb < 0) { prb = arb; col = g0 * 8; } \
        fo = WS_F1 + (prb * 16 + (col >> 7)) * 128; PIN(fo); } while (0)

    if (__builtin_expect(wid >= 8, 1)) {
        const int sw = wid - 8;
        unsigned woo[5];
#pragma unroll
        for (int i = 0; i < 5; ++i) { const int c = i * 64 + lane, row = (c * 6554) >> 16, cc = c - row * 10; woo[i] = row * 1152 + cq * 144 + (cc < 9 ? cc : 8) * 16; PIN(woo[i]); }
        unsigned kv, vv[5], gko;
        unsigned kd = lds0 + L_K + (5 - sw) * 1024, vd = lds0 + L_V + (5 - sw) * 5120, gs_ = 32768u, near = arb | (~sw & 2);
        asm volatile("" : "+s"(kd), "+s"(vd), "+s"(gs_), "+s"(near));
#define KV_ADDR() do { \
        kv = WS_QK + (unsigned)(72 + g0) * 32768u + ((arb * 2 + 1 - sw) * 64 + lane) * 16; \
        _Pragma("unroll") for (int t = 0; t < 5; ++t) { \
            const int c = t * 64 + lane, hb = (c * 1639) >> 16; \
            vv[t] = WS_V + ((arb * 2 + 1 - sw) * 8 + hb) * 9216 + (36 * hp + c - hb * 40) * 16; PIN(vv[t]); } \
        gko = sw == 3 ? WS_AM + (lane & 15) * 128 : WS_G + (rt * 64 + 2 * r32) * 8; \
        PIN(kv); PIN(gko); } while (0)
#define LDK "global_load_lds_dwordx4 %[k], %[ws] sc1\n\t"
#define NXK(op) "s_" op "_u32 m0, m0, 6144\n\tv_" op "_u32 %[k], %[k], %[gs]\n\t"
#define LDV(i, d, lo, hi) "s_add_u32 m0, m0, " #d "\n\ts_mov_b32 exec_lo, " #lo "\n\ts_mov_b32 exec_hi, " #hi "\n\tglobal_load_lds_dwordx4 %[v" #i "], %[vb] sc1\n\t"
#define K_PART(op, bias, dst) asm volatile("s_mov_b32 %[sv], m0\n\ts_mov_b32 m0, %[kd]\n\t" bias \
            LDK NXK(op) LDK NXK(op) LDK NXK(op) LDK NXK(op) LDK "s_mov_b32 m0, %[sv]" \
            : [sv] "=&s"(sv), [k] "+v"(kv) : [ws] "s"(ws), [gs] "s"(gs_), [kd] "s"(dst) : "memory", "scc")
#define V_PART(base, dst) asm volatile("s_mov_b32 %[sv], m0\n\ts_mov_b32 m0, %[vd]\n\t" \
            LDV(0, 0, 0xffffffff, 0xffffff0f) LDV(1, 1024, 0xffff0fff, 0xff0fffff) LDV(2, 1024, 0xfffffff, 0xffffffff) LDV(3, 1024, 0xffffff0f, 0xffff0fff) LDV(4, 1024, 0xff0fffff, 0xfffffff) \
            "s_mov_b64 exec, -1\n\ts_mov_b32 m0, %[sv]" \
            : [sv] "=&s"(sv) : [vb] "s"(base), [vd] "s"(dst), [v0] "v"(vv[0]), [v1] "v"(vv[1]), [v2] "v"(vv[2]), [v3] "v"(vv[3]), [v4] "v"(vv[4]) : "memory", "scc")
#define KV_LOAD() do { unsigned sv; if (near) { K_PART("add", "s_nop 0\n\t", kd); V_PART(ws, vd); } } while (0)
#define KV_FAR() do { unsigned sv; \
        if (arb >= 2 && sw < 2) K_PART("sub", "v_add_u32 %[k], 0xfffff000, %[k]\n\t", lds0 + L_K + 4 * 6144 + (1 - sw) * 1024); \
        if (arb >= 2 && sw >= 2) V_PART(ws - 16 * 9216, lds0 + L_V + (3 - sw) * 5120); } while (0)
#define CLEAR_UNIT() do {        \
            const unsigned t0 = arb * 128 + sw * 32, om = WS_OM + (unsigned)(4 * hp + hh) * 32768u + (t0 + r32) * 16; \
            const v4u e4 = {EMPTY, EMPTY, EMPTY, EMPTY}; \
            st16_dev(om, ws, e4); st16_dev(om + 65536, ws, e4); \
            if ((lane & 3) == 0) st16_dev(WS_OT + (2 * hp + hh) * 8192 + (t0 + r32) * 4, ws, e4); } while (0)
#define X_TILE() _Pragma("unroll") for (int i = 0; i < 3; ++i) {        \
            const int n = i < 2 ? sw + 4 * i : 8, c = n * 64 + lane, row = (c * 7282) >> 16; \
            dma((rt * 64 + row) * 1152 + cq * 144 + (c - row * 9) * 16, x, RFL(lds0 + L_X + n * 1024)); }
        if (__builtin_expect(j < 28, 1)) {
            tag = read_tag(ws);
            BAR();
        }
        if (full) {
            X_TILE();
            wo_blocks<0, 8>(sw, 2 * (rt & 3), woo, wo, lds0);
            wo_blocks<8, 10, 2, 3>(sw, 2 * (rt & 3) + (rt >> 1 & 1), woo, wo, lds0);
            KV_ADDR();
            wait_count<4>(lds + C_TILE);
            CLEAR_UNIT();
        } else {
            unsigned fo, fv = 0;
#define GU_MID if (sw == 0) LD4_DEV(fv, fo, ws)
            if (half) {
                v4u wv[RH / 12];
                GU_ADDR(wid);
                KV_ADDR();
                POLL_ADDR(fo);
                BAR();
                GU_LOAD(RH / 24);
                if (sw == 3) { GET_KEY(); arrive(lds + C_GU); }
                else wait_count<1>(lds + C_GU);
                GU_MUL(RH / 24, wid, GU_MID);
            } else {
                v4u wv[RF / 12];
                GU_ADDR(wid);
                GU_LOAD(RF / 24);
                tag = read_tag(ws);
                KV_ADDR();
                POLL_ADDR(fo);
                if (sw == 3) GET_KEY();
                BAR();
                GU_MUL(RF / 24, wid, GU_MID);
            }
            if (sw == 0) { VMW(0); SBAR(); PIN(fv); wait_flags(fv, fo, ws, tag); }
        }
        BAR();
        KV_LOAD();
        VMW(0);
        BAR();
        KV_FAR();
        if (sw == 0) flag_dev(WS_FC + (arb * 16 + hp) * 128, ws, tag);
        v4u gk;
        LD16_DEV(gk, gko, ws);
        if (sw == 0) VMW(2); else VMW(1);
        arrive(lds + C_KV);
#define LATE_LOADS() do { \
        wo_blocks<8, 10, 0, 1>(sw, 2 * (rt & 3) + (rt >> 1 & 1), woo, wo, lds0); \
        wo_blocks<8, 10, 1, 2>(sw, 2 * (rt & 3) + (rt >> 1 & 1), woo, wo, lds0); \
        if (!full) wo_blocks<8, 10, 2, 3>(sw, 2 * (rt & 3) + (rt >> 1 & 1), woo, wo, lds0); \
        if (!full) { wo_blocks<0, 8>(sw, 2 * (rt & 3), woo, wo, lds0); X_TILE(); } \
        LD4_DEV(fc, fco, ws); } while (0)
#define GATE_KEY() do { \
        SBAR(); PIN(gk); PIN(fc); \
        if (chk && __all(fc == tag)) { arrive(lds + C_ALL); chk = false; } \
        if (sw == 2) { \
            gk = wait16(gk, gko, ws, tag); \
            if (lane < 32) *(LAS v4u*)(lds + L_G + lane * 16) = gk; \
        } \
        if (sw == 3 && full) { \
            const unsigned key_ = wave_umax(wait8((v2u){gk.x, gk.y}, gko, ws, tag).x); \
            if (lane == 0) *(LAS unsigned*)(lds + C_KEY) = key_; \
        } } while (0)
#define TAGGED(v) (v.y == tag && v.w == tag)
        const bool down = b < 162;
        bool chk = sw == 1;
        const unsigned fco = WS_FC + ((rt >> 1) * 16 + (chk ? lane & 15 : 0)) * 128;
        unsigned fc;
        if (!down) {
            LATE_LOADS();
            VMW(0);
            GATE_KEY();
            arrive(lds + C_LATE); arrive(lds + C_ALL);
        } else {
            const int dg = b / 9;
            const unsigned od = (unsigned)(dg * 256 + sw * 64 + (lane >> 3)) * 1152u + (b - dg * 9) * 128 + (lane & 7) * 16;
            v4u dw[8];
            LATE_LOADS();
            arrive(lds + C_WO); wait_count<4>(lds + C_WO);
#pragma unroll
            for (int i = 0; i < 8; ++i) LD16(dw[i], od, wd + i * 9216);
            const int np = first_task(dg * 576 + 575) - first_task(dg * 576) + 1;
            const unsigned o = WS_HP + (dg * 1024 + sw * 64 + lane) * 16, o2 = o + (np > 2 ? 8192 : 0), o3 = o + (np > 3 ? 12288 : 0);
            v4u h0, h1, h2, h3;
            LD16_DEV(h0, o, ws); LD16_DEV(h1, o + 4096, ws); LD16_DEV(h2, o2, ws); LD16_DEV(h3, o3, ws);
            VMW(12);
            GATE_KEY();
            arrive(lds + C_LATE); arrive(lds + C_ALL);
            VMW(0); SBAR(); PIN(h0); PIN(h1); PIN(h2); PIN(h3);
#pragma unroll
            for (int i = 0; i < 8; ++i) PIN(dw[i]);
            int spin = 0;
#pragma unroll 1
            while (__builtin_expect(!__all(TAGGED(h0) && TAGGED(h1) && TAGGED(h2) && TAGGED(h3)), 0) && ++spin < SPIN_CAP) {
                __builtin_amdgcn_s_sleep(3);
                LD16_DEV(h0, o, ws); LD16_DEV(h1, o + 4096, ws); LD16_DEV(h2, o2, ws); LD16_DEV(h3, o3, ws);
                VMW(0); SBAR(); PIN(h0); PIN(h1); PIN(h2); PIN(h3);
            }
            float g = __uint_as_float(h0.x) + __uint_as_float(h1.x), u = __uint_as_float(h0.z) + __uint_as_float(h1.z);
            if (np > 2) { g += __uint_as_float(h2.x); u += __uint_as_float(h2.z); }
            if (np > 3) { g += __uint_as_float(h3.x); u += __uint_as_float(h3.z); }
            g = rbf(g); u = rbf(u);
            const float hv = rbf(g * sigmoidf(g)) * u;
            const unsigned hd2 = cvtpk(hv, hv);
            unsigned hs[8];
#pragma unroll
            for (int i = 0; i < 8; ++i) hs[i] = (unsigned)__builtin_amdgcn_ds_bpermute((8 * i + (lane >> 3)) * 4, (int)hd2);
            v4u m;
#pragma unroll
            for (int rr = 0; rr < 4; ++rr) m[rr] = ((lane & 7) >> 1) == rr ? ((lane & 1) ? 0xffff0000u : 0xffffu) : 0u;
            f32x4 a = {};
#pragma unroll
            for (int i = 0; i < 8; ++i) a = MFMA16(asb((v4u){hs[i] & m[0], hs[i] & m[1], hs[i] & m[2], hs[i] & m[3]}), asb(dw[i]), a);
            if (((lane >> 3) & 1) == hh) *(LAS f32x4*)(lds + (sw * 2 + hh) * 3840 + yslot(2 * (lane & 7) + ((lane >> 4) & 1))) = a;
            if (last_of(lds + C_Y, 4) && lane < 32) {
                f32x2 s = {};
#pragma unroll
                for (int v = 0; v < 8; ++v) s += *(LAS f32x2*)(lds + v * 3840 + yslot(lane >> 1) + (lane & 1) * 8);
                st16_dev(WS_YP + (dg * 288 + (b - dg * 9) * 32 + lane) * 16, ws, (v4u){__float_as_uint(s[0]), tag, __float_as_uint(s[1]), tag});
                arrive(lds + C_Y);
            }
        }
        if (chk) { poll_flags(fco, ws, tag); arrive(lds + C_ALL); }
        if (b == 208 && sw == 3) {
#pragma unroll 1
            for (int i = 0; i < 4; ++i) {
                const int n = i * 64 + lane, t = (n >> 4) * 3 + (n & 1), row = (n >> 4) * 648 + (n & 1) * RF, g = row / 576;
                poll_flags((n & 15) < 14 ? WS_F1 + n * 128 : WS_HP + (g * 4 + t - first_task(g * 576)) * 4096 + 4, ws, tag);
            }
            if (lane == 0) st4_dev(WS_EPOCH, ws, tag);
        }
        wait_count<4>(lds + C_LATE);
        const unsigned key = lds_word(lds + C_KEY);
        if (((0xffff - (key & 0xffff)) >> 6) == rt) {
            const unsigned o = WS_YP + (sw * 288 + cq * 36 + (lane < 36 ? lane : 0)) * 16, o4 = o + (sw < 2 ? 16 * 4608 : 0);
            v4u y0, y1, y2, y3, y4;
            int spin = 0;
#pragma unroll 1
            do {
                if (spin) __builtin_amdgcn_s_sleep(3);
                LD16_DEV(y0, o, ws); LD16_DEV(y1, o + 4 * 4608, ws); LD16_DEV(y2, o + 8 * 4608, ws); LD16_DEV(y3, o + 12 * 4608, ws); LD16_DEV(y4, o4, ws);
                VMW(0); SBAR(); PIN(y0); PIN(y1); PIN(y2); PIN(y3); PIN(y4);
            } while (!__all(TAGGED(y0) && TAGGED(y1) && TAGGED(y2) && TAGGED(y3) && TAGGED(y4)) && ++spin < SPIN_CAP);
            f32x2 s = {(__uint_as_float(y0.x) + __uint_as_float(y1.x)) + (__uint_as_float(y2.x) + __uint_as_float(y3.x)),
                       (__uint_as_float(y0.z) + __uint_as_float(y1.z)) + (__uint_as_float(y2.z) + __uint_as_float(y3.z))};
            if (sw < 2) s += (f32x2){__uint_as_float(y4.x), __uint_as_float(y4.z)};
            if (down) wait_count<5>(lds + C_Y);
            const lptr yp = lds + yslot(lane < 36 ? lane >> 1 : 0) + (lane & 1) * 8;
            if (lane < 36) *(LAS f32x2*)(yp + sw * 3840) = s;
            if (last_of(lds + C_YG, 4)) {
                const unsigned rk = key >> 16, rb16 = (rk & 0x8000u) ? (rk & 0x7fffu) : (~rk & 0xffffu);
                const float rs = rbf(sigmoidf(bflo(rb16)));
                s = (*(LAS f32x2*)yp + *(LAS f32x2*)(yp + 3840)) + (*(LAS f32x2*)(yp + 7680) + *(LAS f32x2*)(yp + 11520));
                *(LAS f32x2*)(yp + 15360) = (f32x2){rbf(s[0]) * rs, rbf(s[1]) * rs};
                arrive(lds + C_YG);
            }
        }
        return;
    }

    const int qt = wid & 3, hd = wid >> 2;
    const int tok0 = arb * 128 + qt * 32;
#define omo (WS_OM + (unsigned)(4 * hp + hh) * 32768u + r32 * 16)
#define oto (WS_OT + 2 * hp * 8192 + r32 * 4)
#define otl (lane < 32 && (lane & 3) == 0)
#define CLEAR_O() do { const v4u e4 = {EMPTY, EMPTY, EMPTY, EMPTY}; st16_dev(omo + hd * 65536 + tok0 * 16, ws, e4); if (otl) st16_dev(oto + hd * 8192 + tok0 * 4, ws, e4); } while (0)
    v4u wv[(RF > RH ? RF : RH) / 12];
    unsigned fo;
    bool poller = false;
    if (j < 28) {
        if (wid < 4) {
            const int wm = wid & 1, wn = wid >> 1;
            if (wid == 0 && lane < 24) *(LAS unsigned*)(lds + C_TILE + lane * 4) = 0u;
            if (ct < 9) gemm_tile<0>(lds, lane, wm, wn, rb, ct, kord, ws);
            else if (full) gemm_tile<1>(lds, lane, wm, wn, rb, ct, kord, ws);
            else gemm_tile<2>(lds, lane, wm, wn, rb, ct, kord, ws);
            PIN(lane); r32 = lane & 31; hh = lane >> 5;
            BAR();
            tag = read_tag(ws);
            unsigned f1o = WS_F1 + (rb * 16 + ct) * 128;
            asm volatile("" : "+v"(f1o) : "s"(out));
            POLL_ADDR(fo);
            VMW(0);
            if (last_of(lds + C_TILE, 4)) { flag_dev(f1o, ws, tag); poller = full; }
        } else {
            unsigned px = 0;
            if (wid == 4 && j < 24 && lane < 32) {
                const int n = cl * 32 + lane, r_ = (n * 171) >> 9;
                asm volatile("global_load_dword %0, %1, %2" : "=v"(px) : "v"((rb * 128 + (xcd & 1) * 64 + r_) * 1152 + (kb2 * 3 + n - 3 * r_) * 128), "s"(x) : "memory");
            }
            int n = ((wid - 4) * 26 + j) * 64 + lane; if (n >= RH * 16) n = RH * 16 - 1;
            const int r_ = (xcd * 2 + ((n >> 3) & 1)) * 648 + 2 * RF + (n >> 4), g_ = (r_ * 58255) >> 25;
            const char* pp = (n & 4 ? wu : wg) + (unsigned)((r_ - g_ * 576) * 9216 + g_ * 512 + (n & 3) * 128);
            if (wid == 7) pp = wo + (unsigned)(b * 25 + (lane < 25 ? lane : 0) < 5184 ? b * 25 + (lane < 25 ? lane : 0) : 0) * 128;
            unsigned pf = 0;
            asm volatile("s_load_dword %0, %1, 0x0\n\ts_barrier\n\ts_barrier\n\ts_barrier\n\ts_barrier\n\ts_barrier\n\ts_barrier\n\ts_barrier\n\ts_waitcnt lgkmcnt(0)"
                         : "=&s"(tag) : "s"(ws) : "memory");
            if (full && ((wid - 4) * 26 * 64 < RH * 16 || wid == 7)) asm volatile("global_load_dword %0, %1, off" : "=v"(pf) : "v"(pp) : "memory");
            asm volatile("s_barrier\n\ts_barrier" ::: "memory");
            ++tag;
            VMW(0); SBAR(); PIN(px); PIN(pf);
            BAR();
        }
        if (half) {
            GU_ADDR(wid);
            CLEAR_O();
            BAR();
            GU_LOAD(RH / 24);
            wait_count<1>(lds + C_GU);
            GU_MUL(RH / 24, wid, );
        }
    } else {
        if (wid == 0 && lane < 24) *(LAS unsigned*)(lds + C_TILE + lane * 4) = 0u;
        GU_ADDR(wid);
        GU_LOAD(RF / 24);
        CLEAR_O();
        tag = read_tag(ws);
        BAR();
        GU_MUL(RF / 24, wid, );
    }

    unsigned f2p;
    {
        if (wid == 7 && lane < 48) *(LAS v4u*)(lds + L_V + lane * 640 + 576) = (v4u){0x3f803f80u, 0x3f803f80u, 0x3f803f80u, 0x3f803f80u};
        unsigned qo0 = WS_QK + (unsigned)(g0 + 2 * hd + hh) * 32768u + (tok0 + r32) * 16, qo1 = WS_QK + (unsigned)(g0 + 2 * hd + 2) * 32768u + (tok0 + r32) * 16;
        asm volatile("" : "+v"(qo0), "+v"(qo1) : "s"(out));
        if (poller) { __builtin_amdgcn_s_sleep(18); poll_flags(fo, ws, tag); }
        BAR();
        v4u q0, q1;
        LD16_DEV(q0, qo0, ws);
        asm volatile("s_mov_b32 exec_hi, 0\n\tglobal_load_dwordx4 %0, %1, %2 sc1\n\ts_mov_b32 exec_hi, -1" : "=v"(q1) : "v"(qo1), "s"(ws) : "memory");
        const bool pub = !full && (hd == 0 || 576 - gk0 < (half ? RH : RF));
        if (pub) GU_PUBLISH(hd, qt);
        f32x16 mA, mB;
#pragma unroll
        for (int r = 0; r < 16; ++r) {
            mA[r] = (r & 3) + 8 * (r >> 2) + 4 * hh <= r32 ? 0.f : -INFINITY;
            mB[r] = __uint_as_float(__float_as_uint(mA[r]) ^ 0xff800000u);
        }
        lptr kp0 = lds + L_K + (2 * hd + hh) * 6144 + (qt * 32 + r32) * 16;
        lptr kp1 = lds + L_K + (2 * hd + 2) * 6144 + (qt * 32 + r32) * 16;
        lptr vp = lds + L_V + (qt * 4 + hh) * 640 + ((r32 == 18 || r32 == 22) ? 36 : 18 * hd + r32) * 16;
        unsigned so = omo + hd * 65536 + tok0 * 16, sto = oto + hd * 8192 + tok0 * 4, f2o = WS_F2 + (arb * 16 + hp) * 128;
        f2p = WS_F2 + ((rt >> 1) * 16 + (lane & 15)) * 128; PIN(f2p);
        v4u z0, z1;
        const int a0 = (36 * hp + 18 * hd) & 7;
#pragma unroll
        for (int e = 0; e < 4; ++e) { z0[e] = hh == 0 && 2 * e < a0 ? 0u : ~0u; z1[e] = hh == 1 || 2 * e >= a0 + 2 ? 0u : ~0u; }
        PIN(mA); PIN(mB); PIN(kp0); PIN(kp1); PIN(vp); PIN(so); PIN(sto); PIN(f2o); PIN(z0); PIN(z1);
        if (pub) VMW(1); else VMW(0);
        SBAR(); PIN(q0); PIN(q1);
        q0 &= z0; q1 &= z1;
        PIN(q0); PIN(q1);
        const f32x16 z = {};
        f32x16 o, sa, sb;
        bf16x8 k0, k1, v0, v1, bq0 = asb(q0), bq1 = asb(q1);
        v4u p0, p1;
        f32x4 et, eu;
        BAR();
#define EX2(s, r) cvtpk(__builtin_amdgcn_exp2f(s[r]), __builtin_amdgcn_exp2f(s[r + 1]))
#define HEAD() do { \
        k0 = lds16(kp0 + 8 * 512); k1 = lds16(kp1 + 8 * 512); SBAR(); \
        v0 = lds16(vp + 8 * 2560); SBAR(); \
        sa = MFMA32(k0, bq0, mA); k0 = lds16(kp0 + 7 * 512); sa = MFMA32(k1, bq1, sa); k1 = lds16(kp1 + 7 * 512); o = z; SBAR(); } while (0)
#define GAP(n, SC, r, P, c, m, PK) do { \
        n[0] = __builtin_amdgcn_exp2f(SC[r]); n[1] = __builtin_amdgcn_exp2f(SC[r + 1]); SBAR(); \
        if (PK) { P[c] = cvtpk(m[0], m[1]); P[c + 1] = cvtpk(m[2], m[3]); PIN(P); } SBAR(); \
        n[2] = __builtin_amdgcn_exp2f(SC[r + 2]); n[3] = __builtin_amdgcn_exp2f(SC[r + 3]); PIN(n); SBAR(); } while (0)
#define STEP(i, SC, SN, CN, PEND, MID) do { \
        SN = MFMA32(k0, bq0, CN); SBAR(); \
        if ((i) >= 2) k0 = lds16(kp0 + ((i) - 2) * 512); GAP(eu, SC, 0, p1, 2, et, PEND); \
        if (PEND) o = MFMA32(v1, asb(p1), o); SBAR(); \
        v1 = lds16(vp + (i) * 2560 + 1280); GAP(et, SC, 4, p0, 0, eu, true); \
        SN = MFMA32(k1, bq1, SN); SBAR(); \
        if ((i) >= 2) k1 = lds16(kp1 + ((i) - 2) * 512); MID; GAP(eu, SC, 8, p0, 2, et, true); \
        o = MFMA32(v0, asb(p0), o); SBAR(); \
        v0 = lds16(vp + ((i) - 1) * 2560); GAP(et, SC, 12, p1, 0, eu, true); } while (0)
#define TAIL(i) do { \
        GAP(eu, sa, 0, p1, 2, et, true); \
        o = MFMA32(v1, asb(p1), o); SBAR(); \
        v1 = lds16(vp + (i) * 2560 + 1280); GAP(et, sa, 4, p0, 0, eu, true); GAP(eu, sa, 8, p0, 2, et, true); \
        o = MFMA32(v0, asb(p0), o); SBAR(); \
        GAP(et, sa, 12, p1, 0, eu, true); p1[2] = cvtpk(et[0], et[1]); p1[3] = cvtpk(et[2], et[3]); PIN(p1); SBAR(); \
        o = MFMA32(v1, asb(p1), o); } while (0)
#define FINISH(imin) do { \
        float inv = __builtin_amdgcn_rcpf(o[10]); PIN(inv); \
        if (__builtin_expect(__any(!(o[10] >= 0x1p-64f && o[10] <= 0x1p64f)), 0)) { \
            float mx = -INFINITY; \
            o = z; \
            _Pragma("unroll 1") for (int it = (imin) - 9; it <= 8; ++it) { \
                if (it < (imin) && it >= 0) it = (imin); \
                const int i = it < 0 ? it + 9 : it; \
                if (it == (imin)) mx = fmaxf(mx, __shfl_xor(mx, 32)); \
                f32x16 s = MFMA32(lds16(kp0 + i * 512), bq0, z); s = MFMA32(lds16(kp1 + i * 512), bq1, s); \
                _Pragma("unroll") for (int r = 0; r < 16; ++r) { \
                    const int key = (r & 3) + 8 * (r >> 2) + 4 * hh; \
                    if ((i == 8 && key > r32) || (i == 0 && key <= r32)) s[r] = -INFINITY; \
                } \
                if (it < 0) { \
                    _Pragma("unroll") for (int r = 0; r < 16; ++r) mx = fmaxf(mx, s[r]); \
                } else { \
                    _Pragma("unroll") for (int r = 0; r < 16; ++r) s[r] -= mx; \
                    const v4u e0 = {EX2(s, 0), EX2(s, 2), EX2(s, 4), EX2(s, 6)}, e1 = {EX2(s, 8), EX2(s, 10), EX2(s, 12), EX2(s, 14)}; \
                    o = MFMA32(lds16(vp + i * 2560), asb(e0), o); o = MFMA32(lds16(vp + i * 2560 + 1280), asb(e1), o); \
                } \
            } \
            inv = __builtin_amdgcn_rcpf(o[10]); \
        } \
        v4u dd; \
        const unsigned tl = umin(cvtpk(o[8] * inv, o[9] * inv), EMPTY - 1); \
        _Pragma("unroll") for (int i = 0; i < 4; ++i) dd[i] = umin(cvtpk(o[2 * i] * inv, o[2 * i + 1] * inv), EMPTY - 1); \
        v4u t4 = {tl, DPP(tl, 0x55), DPP(tl, 0xAA), DPP(tl, 0xFF)}; \
        asm volatile("" : "+v"(t4), "+v"(dd)); \
        st16_dev(so, ws, dd); \
        asm volatile("s_mov_b64 exec, 0x11111111\n\tglobal_store_dwordx4 %0, %1, %2 sc1\n\ts_mov_b64 exec, -1\n\ts_nop 0" :: "v"(sto), "v"(t4), "s"(ws) : "memory"); } while (0)

#define HINT() do { if (wid == 7) flag_dev(f2o, ws, tag); } while (0)
        if (arb >= 2) {
            unsigned kvc;
            HEAD();
            STEP(8, sa, sb, z, false, );
            STEP(7, sb, sa, z, true, );
            STEP(6, sa, sb, z, true, kvc = *(volatile LAS unsigned*)(lds + C_KV));
            if (__builtin_expect(RFL(kvc) != 4, 0)) wait_count<4>(lds + C_KV);
            STEP(5, sb, sa, z, true, );
            STEP(4, sa, sb, z, true, );
            STEP(3, sb, sa, z, true, );
            STEP(2, sa, sb, z, true, );
            HINT();
            STEP(1, sb, sa, mB, true, );
            TAIL(0);
            FINISH(0);
        } else {
            const int imin = 8 - 4 * arb - qt;
            HEAD();
            p1 = (v4u){0u, 0u, 0u, 0u}; v1 = v0; et = (f32x4){};
            do {
                if (imin == 8) break;
                STEP(8, sa, sb, z, true, ); if (imin == 7) { sa = sb; break; }
                STEP(7, sb, sa, z, true, ); if (imin == 6) break;
                STEP(6, sa, sb, z, true, ); if (imin == 5) { sa = sb; break; }
                STEP(5, sb, sa, z, true, ); if (imin == 4) break;
                STEP(4, sa, sb, z, true, ); if (imin == 3) { sa = sb; break; }
                STEP(3, sb, sa, z, true, ); if (imin == 2) break;
                STEP(2, sa, sb, z, true, ); sa = sb;
            } while (0);
            HINT();
            TAIL(imin);
            FINISH(imin);
        }
    }
    arrive(lds + C_ALL);
    const bool watch = last_of(lds + C_ATT, 8);

    const int t16 = lane & 15, g4 = lane >> 4, rq = (wid + cq) & 3;
    const int trow = rq * 16 + t16, tok = rt * 64 + trow;
    bf16x8 wf[9][5];
    lptr wp = lds + L_WO + (18 * (hd + 2 * (g4 >> 1)) + 4 * (g4 & 1) + (t16 >> 2)) * 160 + (lane & 3) * 8;
    const lptr wt = lds + L_WO + (18 * (hd + 8 * g4 + 2 * (t16 >> 3)) + 16 + ((t16 >> 2) & 1)) * 160 + (lane & 3) * 8;
#define WF(s, a, d) _Pragma("unroll") for (int c = 0; c < 5; ++c) wf[s][c] = cat(ldstr(a + c * 32), ldstr(a + c * 32 + (d) * 160)); SBAR();
#define WFRAG(s) { const lptr a_ = wp + (((s) + cq) & 7) * (72 * 160); WF(s, a_, 8) }
#define OADDR(s) (ws + WS_OM + (((s) + cq) & 7) * 262144)
#define TADDR(e) (ws + WS_OT + (e) * 16384)
#define ISBAD(v) __builtin_expect(__any(umax(umax(v.x, v.y), umax(v.z, v.w)) == EMPTY), 0)
#define OFIX(s) if (ISBAD(of[s])) { int spin = 0; _Pragma("unroll 1") do { __builtin_amdgcn_s_sleep(2); of[s] = ld16_dev_wait(omv, OADDR(s)); } while (ISBAD(of[s]) && ++spin < SPIN_CAP); }
#define OWAIT(s, n) VMW(n); SBAR(); PIN(of[s]); OFIX(s)
#define MX4(m, v) umax(umax(umax(umax(m, v.x), v.y), v.z), v.w)
#define OWAIT2(s, n) VMW(n); SBAR(); PIN(of[s]); PIN(of[s + 1]); \
        if (__builtin_expect(__any(MX4(MX4(0u, of[s]), of[s + 1]) == EMPTY), 0)) { OFIX(s) OFIX(s + 1) }
#define OWAIT3(s, n) VMW(n); SBAR(); PIN(of[s]); PIN(of[s + 1]); PIN(of[s + 2]); \
        if (__builtin_expect(__any(MX4(MX4(MX4(0u, of[s]), of[s + 1]), of[s + 2]) == EMPTY), 0)) { OFIX(s) OFIX(s + 1) OFIX(s + 2) }
#define RL(s) ((s) >= 1 && (s) <= 5)
#define OM(s, c, a, d) acc[c] = MFMA16(wf[s][c], asb(of[s]), acc[c]); SBAR(); \
        if (RL(s)) { wf[RL(s) ? (s) + 3 : 0][c] = cat(ldstr(a + (c) * 32), ldstr(a + (c) * 32 + (d) * 160)); SBAR(); }
#define OSTEP(s, NEXT) { const lptr a_ = (s) + 3 < 8 ? wp + (((s) + 3 + cq) & 7) * (72 * 160) : wt; constexpr int d_ = (s) + 3 < 8 ? 8 : 72; \
        OM(s, 0, a_, d_) OM(s, 1, a_, d_) OM(s, 2, a_, d_) OM(s, 3, a_, d_) NEXT; SBAR(); OM(s, 4, a_, d_) }
    f32x4 acc[5], bb[5];
    unsigned selkey, exw, other; float g;
    unsigned omv = (unsigned)(2 * hd + 4 * (g4 >> 1) + (g4 & 1)) * 32768u + tok * 16, otv = (hd + 8 * g4) * 8192 + tok * 4;
    u64 oa0 = (u64)OADDR(0), oa1 = (u64)OADDR(1), oa2 = (u64)OADDR(2);
    lptr ex = lds + rq * 5120 + lane * 16, exf = lds + C_EX + rq * 4;
    unsigned oo = tok * 1152 + cq * 144 + 32 * (g4 & 1) + 16 * (g4 >> 1), oo2 = tok * 1152 + cq * 144 + 128 + 8 * g4;
    u64 outp = (u64)out;
    asm volatile("" : "+v"(omv), "+v"(otv), "+v"(wp), "+v"(ex), "+v"(exf), "+v"(oo), "+v"(oo2));
    asm volatile("" : "+s"(oa0), "+s"(oa1), "+s"(oa2), "+s"(outp));
    unsigned f2v;
    if (watch) LD4_DEV(f2v, f2p, ws);
    wait_count<13>(lds + C_ALL);
    if (hd) { WFRAG(0) WFRAG(1) WFRAG(2) WFRAG(3) }
    if (watch) { VMW(0); SBAR(); PIN(f2v); wait_flags(f2v, f2p, ws, tag); arrive(lds + C_GO); }
    else if (hd) wait_count<1>(lds + C_GO);
    {
        v4u of[9];
        unsigned ot[4];
        LD16_DEV(of[0], omv, oa0); LD16_DEV(of[1], omv, oa1); LD16_DEV(of[2], omv, oa2);
        SBAR(); if (!hd) WFRAG(0)
        LD16_DEV(of[3], omv, OADDR(3)); LD16_DEV(of[4], omv, OADDR(4)); LD16_DEV(of[5], omv, OADDR(5));
        SBAR(); if (!hd) WFRAG(1)
        LD16_DEV(of[6], omv, OADDR(6)); LD16_DEV(of[7], omv, OADDR(7));
#pragma unroll
        for (int e = 0; e < 4; ++e) LD4_DEV(ot[e], otv, TADDR(e));
        SBAR();
        selkey = *(LAS unsigned*)(lds + C_KEY);
        g = *(LAS float*)(lds + L_G + trow * 8);
        SBAR();
        if (!hd) WFRAG(2) if (!hd) WFRAG(3)
        selkey = RFL(selkey);
        other = ((0xffff - (selkey & 0xffff)) >> 4) ^ (rt * 4 + rq) ^ ((hd ^ 1) << 16);
        asm volatile("" : "+s"(other));
#pragma unroll
        for (int c = 0; c < 5; ++c) acc[c] = (f32x4){};
        OWAIT(0, 11)
        OSTEP(0, OWAIT2(1, 9)) OSTEP(1, ) OSTEP(2, OWAIT2(3, 7)) OSTEP(3, ) OSTEP(4, OWAIT3(5, 4)) OSTEP(5, )
        asm volatile("" ::: "memory");
        if (hd) {
            exw = *(volatile LAS unsigned*)exf;
            asm volatile("" ::: "memory");
#pragma unroll
            for (int c = 0; c < 5; ++c) bb[c] = *(LAS f32x4*)(ex + c * 1024);
        } else {
#pragma unroll
            for (int c = 0; c < 5; ++c) {
                const v2u xr = *(LAS v2u*)(lds + L_X + trow * 144 + (c < 4 ? 32 * c + 8 * g4 : 128 + 8 * (g4 & 1)));
                bb[c] = (1.f - g) * (f32x4){bflo(xr.x), bfhi(xr.x), bflo(xr.y), bfhi(xr.y)};
            }
            exw = 1u;
        }
        SBAR();
#define EXWAIT() { \
        unsigned c_, spin = -16 * SPIN_CAP; \
        asm volatile("s_branch 3f\n1:\n\tds_read_b32 %0, %8\n\tds_read_b128 %3, %9\n\tds_read_b128 %4, %9 offset:1024\n\tds_read_b128 %5, %9 offset:2048\n\t" \
                     "ds_read_b128 %6, %9 offset:3072\n\tds_read_b128 %7, %9 offset:4096\n\ts_waitcnt lgkmcnt(0)\n" \
                     "3:\n\tv_readfirstlane_b32 %1, %0\n\ts_cmp_eq_u32 %1, 1\n\ts_cbranch_scc1 2f\n\ts_add_u32 %2, %2, 1\n\ts_cbranch_scc0 1b\n2:" \
                     : "+v"(exw), "=&s"(c_), "+s"(spin), "+v"(bb[0]), "+v"(bb[1]), "+v"(bb[2]), "+v"(bb[3]), "+v"(bb[4]) : "v"(exf), "v"(ex) : "memory", "scc"); \
        SBAR(); }
#define TWAIT() VMW(0); SBAR(); PIN(ot[0]); PIN(ot[1]); PIN(ot[2]); PIN(ot[3]); \
        of[8] = (v4u){ot[0], ot[1], ot[2], ot[3]}; \
        if (ISBAD(of[8])) { int spin = 0; _Pragma("unroll 1") do { __builtin_amdgcn_s_sleep(2); \
                of[8] = (v4u){ld4_dev_wait(otv, TADDR(0)), ld4_dev_wait(otv, TADDR(1)), ld4_dev_wait(otv, TADDR(2)), ld4_dev_wait(otv, TADDR(3))}; \
            } while (ISBAD(of[8]) && ++spin < SPIN_CAP); }
        OSTEP(6, ) OSTEP(7, TWAIT()) EXWAIT() OSTEP(8, )
    }
    {
        const int sel = 0xffff - (selkey & 0xffff);
        if (__builtin_expect(other == 0, 0)) {
            wait_count<5>(lds + C_YG);
#pragma unroll
            for (int c = 0; c < 5; ++c) {
                const f32x4 yy = *(LAS f32x4*)(lds + 15360 + yslot(c < 4 ? 4 * c + g4 : 16 + (g4 & 1)));
#pragma unroll
                for (int e = 0; e < 4; ++e) bb[c][e] = fmaf(1.f - g, tok == sel ? yy[e] : 0.f, bb[c][e]);
            }
        }
#pragma unroll
        for (int c = 0; c < 5; ++c) { bb[c] += g * acc[c]; PIN(bb[c]); }
    }
    if (hd == 0) {
#pragma unroll
        for (int c = 0; c < 5; ++c) *(LAS f32x4*)(ex + c * 1024) = bb[c];
        asm volatile("" ::: "memory");
        *(volatile LAS unsigned*)exf = 1u;
    } else {
        v2u r[5];
#pragma unroll
        for (int c = 0; c < 5; ++c) r[c] = (v2u){cvtpk(bb[c][0], bb[c][1]), cvtpk(bb[c][2], bb[c][3])};
        v4u o4[2];
#pragma unroll
        for (int c = 0; c < 2; ++c) {
            auto s0 = __builtin_amdgcn_permlane16_swap(r[2 * c].x, r[2 * c + 1].x, false, false);
            auto s1 = __builtin_amdgcn_permlane16_swap(r[2 * c].y, r[2 * c + 1].y, false, false);
            o4[c] = (v4u){s0[0], s1[0], s0[1], s1[1]};
        }
        asm volatile("global_store_dwordx4 %0, %1, %5\n\tglobal_store_dwordx4 %0, %2, %5 offset:64\n\ts_mov_b32 exec_hi, 0\n\tglobal_store_dwordx2 %3, %4, %5\n\ts_mov_b32 exec_hi, -1"
                     :: "v"(oo), "v"(o4[0]), "v"(o4[1]), "v"(oo2), "v"(r[4]), "s"(outp) : "memory");
    }
}

extern "C" void kernel_launch(void* const* d_in, const int* in_sizes, int n_in, void* d_out, int out_size, void* d_ws, size_t ws_size,
                              hipStream_t stream) {
    fused_kernel<<<256, 768, 0, stream>>>((const char*)d_in[0], (const char*)d_in[1], (char*)d_ws, (const char*)d_in[5], (const char*)d_in[6],
                                          (const char*)d_in[4], (const char*)d_in[8], (const char*)d_in[2], (const char*)d_in[7], (char*)d_out);
}
```
